```python
import torch

def _softmax(x, axis=-1):
    return torch.softmax(x, dim=axis)
def _gelu(x):
    return torch.nn.functional.gelu(x, approximate="tanh")
def _silu(x):
    return torch.nn.functional.silu(x)
def _sigmoid(x):
    return torch.sigmoid(x)
def _layernorm(x, axis=-1, eps=1e-5):
    m = x.mean(dim=axis, keepdim=True)
    v = x.var(dim=axis, keepdim=True, unbiased=False)
    return (x - m) / torch.sqrt(v + eps)
def _scatter_add(tgt, idx, src, axis=0):
    return tgt.clone().index_add_(axis, idx, src)
def _segment_sum(src, seg, n):
    out = torch.zeros((n,) + src.shape[1:], dtype=src.dtype)
    return out.index_add_(0, seg, src)
def _pad(x, pw):
    flat = tuple(v for pair in reversed(pw) for v in pair)
    return torch.nn.functional.pad(x, flat)

kernel_name = 'standalone__weight_only_int_quip_2__prompt'

def setup_inputs():

    _w = (lambda a: (lambda t: t.to(torch.bfloat16) if t.is_floating_point() else t)(torch.as_tensor(a)))
    B, S, D, F = 1, 2048, 576, 4608
    nbits = max(2, min(4, int(3)))
    G = 32 if 32 > 0 else D
    D = (D // G) * G
    ng = D // G
    qmax = 1 << nbits
    qw = torch.randint(0, qmax, (D, F)).to(torch.int32)
    zp = torch.randint(0, qmax, (ng, F)).to(float)
    sc = torch.abs((lambda *s: torch.randn(*s, dtype=torch.bfloat16) * 0.05)(ng, F)).to(float) * 0.1 + 0.02
    return {
        "x": _w((lambda *s: torch.randn(*s, dtype=torch.bfloat16) * 0.05)(B, S, D)),
        "qw": _w(qw),
        "scales": _w(sc),
        "zeros": _w(zp),
    }

def reference(x, qw, scales, zeros):
    B, S, D = x.shape
    ng, F = scales.shape
    G = D // ng
    s = (lambda x, n, axis: torch.repeat_interleave(x, n, dim=axis))(scales, G, axis=0)
    z = (lambda x, n, axis: torch.repeat_interleave(x, n, dim=axis))(zeros, G, axis=0)
    w = ((lambda x, dt: x.to(dt))(qw, x.dtype) - z) * s
    return torch.einsum("bsd,df->bsf", x, w)
```

```cpp
#include <hip/hip_runtime.h>
#include <hip/hip_bf16.h>
typedef unsigned short u16;
__device__ inline float bf2f(u16 v){ return __uint_as_float(((unsigned)v)<<16); }
__device__ inline u16 f2bf(float f){ unsigned u=__float_as_uint(f); return (u16)((u + 0x7FFF + ((u>>16)&1))>>16); }
__global__ void naive_kernel(const u16* x,const int* qw,const u16* sc,const u16* zp,u16* out,int M,int K,int N){
  int n = blockIdx.x*64 + (threadIdx.x&63);
  int m0 = blockIdx.y*16 + (threadIdx.x>>6)*4;
  float acc[4]={0,0,0,0};
  for(int k=0;k<K;++k){
    int g=k>>5;
    float d=bf2f(f2bf((float)qw[k*N+n]-bf2f(zp[g*N+n])));
    float w=bf2f(f2bf(d*bf2f(sc[g*N+n])));
    for(int i=0;i<4;++i) acc[i]+=bf2f(x[(m0+i)*K+k])*w;
  }
  for(int i=0;i<4;++i) out[(m0+i)*N+n]=f2bf(acc[i]);
}
extern "C" void kernel_launch(void* const* d_in, const int* in_sizes, int n_in,
                              void* d_out, int out_size, void* d_ws, size_t ws_size,
                              hipStream_t stream) {
  const int M=2048,K=576,N=4608;
  naive_kernel<<<dim3(N/64,M/16),256,0,stream>>>((const u16*)d_in[0],(const int*)d_in[1],(const u16*)d_in[2],(const u16*)d_in[3],(u16*)d_out,M,K,N);
}
```

```cpp
#include <hip/hip_runtime.h>

__global__ __launch_bounds__(512, 2) void wq_gemm_kernel(const void* x, const void* qw, const void* sc, const void* zp, void* out) {
    __shared__ __attribute__((aligned(16))) char smem[163840];
    unsigned tid = threadIdx.x;
    const void* ka = (const void*)__builtin_amdgcn_kernarg_segment_ptr();
    unsigned lds0 = (unsigned)(size_t)(__attribute__((address_space(3))) char*)smem;
    asm volatile(
        "L_begin_%=:\n\t"
        "s_load_dwordx8 s[4:11], %[ka], 0x0\n\t"
        "s_load_dwordx2 s[12:13], %[ka], 0x20\n\t"
        "v_lshrrev_b32 v1, 6, v0\n\t"
        "v_and_b32 v0, 63, v0\n\t"
        "v_readfirstlane_b32 s56, v1\n\t"
        "s_and_b32 s62, %[blk], 7\n\t"
        "s_lshr_b32 s63, %[blk], 3\n\t"
        "s_lshl_b32 s64, s62, 5\n\t"
        "s_max_i32 s65, s62, 4\n\t"
        "s_sub_i32 s65, s65, 4\n\t"
        "s_add_i32 s64, s64, s63\n\t"
        "s_sub_i32 s64, s64, s65\n\t"
        "s_mul_i32 s57, s64, 9363\n\t"
        "s_lshr_b32 s57, s57, 16\n\t"
        "s_mul_i32 s65, s57, 7\n\t"
        "s_sub_i32 s58, s64, s65\n\t"
        "s_mul_i32 s59, s58, 18\n\t"
        "s_min_u32 s65, s58, 2\n\t"
        "s_add_i32 s59, s59, s65\n\t"
        "s_cmp_lt_u32 s58, 2\n\t"
        "s_cselect_b32 s60, 19, 18\n\t"
        "s_cmp_lt_u32 s56, 4\n\t"
        "s_cbranch_scc1 L_cons_%=\n\t"
        "s_add_i32 s62, s57, s58\n\t"
        "s_mul_i32 s63, s62, 16384\n\t"
        "s_lshr_b32 s63, s63, 16\n\t"
        "s_mul_i32 s63, s63, 4\n\t"
        "s_sub_i32 s62, s62, s63\n\t"
        "s_mul_i32 s16, s62, 256\n\t"
        "s_add_i32 s17, s16, 128\n\t"
        "s_cmp_eq_u32 s17, 1152\n\t"
        "s_cselect_b32 s17, 0, s17\n\t"
        "s_add_i32 s18, s17, 128\n\t"
        "s_cmp_eq_u32 s18, 1152\n\t"
        "s_cselect_b32 s18, 0, s18\n\t"
        "s_add_i32 s19, s18, 128\n\t"
        "s_cmp_eq_u32 s19, 1152\n\t"
        "s_cselect_b32 s19, 0, s19\n\t"
        "s_add_i32 s20, s19, 128\n\t"
        "s_cmp_eq_u32 s20, 1152\n\t"
        "s_cselect_b32 s20, 0, s20\n\t"
        "s_add_i32 s21, s20, 128\n\t"
        "s_cmp_eq_u32 s21, 1152\n\t"
        "s_cselect_b32 s21, 0, s21\n\t"
        "s_add_i32 s22, s21, 128\n\t"
        "s_cmp_eq_u32 s22, 1152\n\t"
        "s_cselect_b32 s22, 0, s22\n\t"
        "s_add_i32 s23, s22, 128\n\t"
        "s_cmp_eq_u32 s23, 1152\n\t"
        "s_cselect_b32 s23, 0, s23\n\t"
        "s_add_i32 s24, s23, 128\n\t"
        "s_cmp_eq_u32 s24, 1152\n\t"
        "s_cselect_b32 s24, 0, s24\n\t"
        "s_sub_i32 s56, s56, 4\n\t"
        "s_and_b32 s62, s56, 1\n\t"
        "s_lshr_b32 s76, s56, 1\n\t"
        "v_lshrrev_b32 v1, 3, v0\n\t"
        "v_and_b32 v2, 7, v0\n\t"
        "v_lshrrev_b32 v3, 4, v0\n\t"
        "v_and_b32 v104, 15, v0\n\t"
        "v_mul_u32_u24 v129, 18432, v1\n\t"
        "v_lshl_add_u32 v129, v2, 4, v129\n\t"
        "v_mul_u32_u24 v112, 147456, v3\n\t"
        "v_lshl_add_u32 v112, v104, 4, v112\n\t"
        "v_add_u32 v113, 18432, v112\n\t"
        "v_add_u32 v114, 36864, v112\n\t"
        "v_add_u32 v115, 55296, v112\n\t"
        "v_add_u32 v116, 73728, v112\n\t"
        "v_add_u32 v117, 92160, v112\n\t"
        "v_add_u32 v118, 110592, v112\n\t"
        "v_add_u32 v119, 129024, v112\n\t"
        "s_mul_i32 s63, s76, 9216\n\t"
        "s_lshl_b32 s64, s57, 8\n\t"
        "s_add_i32 s63, s63, s64\n\t"
        "s_lshl_b32 s64, s62, 7\n\t"
        "s_add_i32 s63, s63, s64\n\t"
        "s_add_i32 s64, s63, 147456\n\t"
        "s_mul_i32 s65, s56, 2304\n\t"
        "s_add_i32 s65, s65, 94208\n\t"
        "s_mul_i32 s90, s76, 589824\n\t"
        "s_lshl_b32 s66, s57, 9\n\t"
        "s_add_i32 s90, s90, s66\n\t"
        "s_lshl_b32 s66, s62, 8\n\t"
        "s_add_i32 s90, s90, s66\n\t"
        "s_mov_b32 m0, s65\n\t"
        "s_waitcnt lgkmcnt(0)\n\t"
        "s_and_b32 s45, s9, 0xffff\n\t"
        "s_mov_b32 s44, s8\n\t"
        "s_mov_b32 s46, 165888\n\t"
        "s_mov_b32 s47, 0x20000\n\t"
        "s_and_b32 s41, s7, 0xffff\n\t"
        "s_mov_b32 s40, s6\n\t"
        "s_mov_b32 s42, 10616832\n\t"
        "s_mov_b32 s43, 0x20000\n\t"
        "buffer_load_dwordx4 v129, s[44:47], s63 offen lds\n\t"
        "s_mul_i32 s72, s16, 9216\n\t"
        "s_add_i32 s72, s72, s90\n\t"
        "buffer_load_dwordx4 v[4:7], v112, s[40:43], s72 offen \n\t"
        "buffer_load_dwordx4 v[8:11], v113, s[40:43], s72 offen \n\t"
        "buffer_load_dwordx4 v[12:15], v114, s[40:43], s72 offen \n\t"
        "buffer_load_dwordx4 v[16:19], v115, s[40:43], s72 offen \n\t"
        "buffer_load_dwordx4 v[20:23], v116, s[40:43], s72 offen \n\t"
        "buffer_load_dwordx4 v[24:27], v117, s[40:43], s72 offen \n\t"
        "buffer_load_dwordx4 v[28:31], v118, s[40:43], s72 offen \n\t"
        "buffer_load_dwordx4 v[32:35], v119, s[40:43], s72 offen \n\t"
        "s_and_b32 s49, s11, 0xffff\n\t"
        "s_mov_b32 s48, s10\n\t"
        "s_mov_b32 s50, 165888\n\t"
        "s_mov_b32 s51, 0x20000\n\t"
        "s_add_i32 s66, s65, 1152\n\t"
        "s_mov_b32 m0, s66\n\t"
        "s_add_i32 s67, s65, 1024\n\t"
        "buffer_load_dwordx4 v129, s[48:51], s63 offen lds\n\t"
        "s_mov_b32 m0, s67\n\t"
        "s_mov_b64 exec, 0xff\n\t"
        "buffer_load_dwordx4 v129, s[44:47], s64 offen lds\n\t"
        "s_add_i32 s67, s66, 1024\n\t"
        "s_mov_b32 m0, s67\n\t"
        "s_nop 0\n\t"
        "buffer_load_dwordx4 v129, s[48:51], s64 offen lds\n\t"
        "s_mov_b64 exec, -1\n\t"
        "s_and_b32 s37, s5, 0xffff\n\t"
        "s_mov_b32 s36, s4\n\t"
        "s_mov_b32 s38, 2359296\n\t"
        "s_mov_b32 s39, 0x20000\n\t"
        "s_lshl_b32 s63, s62, 2\n\t"
        "v_add_u32 v105, s63, v3\n\t"
        "v_xor_b32 v105, v105, v2\n\t"
        "v_mul_u32_u24 v120, 1152, v1\n\t"
        "v_lshl_add_u32 v120, v105, 4, v120\n\t"
        "v_add_u32 v121, 35840, v120\n\t"
        "v_add_u32 v122, 71680, v120\n\t"
        "v_add_u32 v123, 107520, v120\n\t"
        "v_add_u32 v124, 147456, v120\n\t"
        "s_lshl_b32 s63, s59, 1\n\t"
        "s_lshl_b32 s64, s76, 1\n\t"
        "s_add_i32 s63, s63, s64\n\t"
        "s_add_i32 s63, s63, s62\n\t"
        "s_mul_i32 s91, s63, 9216\n\t"
        "s_mul_i32 s75, s56, 5120\n\t"
        "s_add_i32 m0, s75, 0\n\t"
        "s_add_i32 s73, s91, s16\n\t"
        "buffer_load_dwordx4 v120, s[36:39], s73 offen  lds\n\t"
        "buffer_load_dwordx4 v121, s[36:39], s73 offen offset:1024  lds\n\t"
        "buffer_load_dwordx4 v122, s[36:39], s73 offen offset:2048  lds\n\t"
        "buffer_load_dwordx4 v123, s[36:39], s73 offen offset:3072  lds\n\t"
        "s_add_i32 m0, s75, 4096\n\t"
        "s_nop 0\n\t"
        "buffer_load_dwordx4 v124, s[36:39], s73 offen  lds\n\t"
        "s_sub_i32 s63, s60, 10\n\t"
        "s_lshl_b32 s64, s57, 1\n\t"
        "s_mul_i32 s66, s63, 8\n\t"
        "s_cmp_ge_u32 s64, s66\n\t"
        "s_cselect_b32 s66, s66, 0\n\t"
        "s_sub_i32 s64, s64, s66\n\t"
        "s_mul_i32 s66, s63, 4\n\t"
        "s_cmp_ge_u32 s64, s66\n\t"
        "s_cselect_b32 s66, s66, 0\n\t"
        "s_sub_i32 s64, s64, s66\n\t"
        "s_mul_i32 s66, s63, 2\n\t"
        "s_cmp_ge_u32 s64, s66\n\t"
        "s_cselect_b32 s66, s66, 0\n\t"
        "s_sub_i32 s64, s64, s66\n\t"
        "s_mul_i32 s66, s63, 1\n\t"
        "s_cmp_ge_u32 s64, s66\n\t"
        "s_cselect_b32 s66, s66, 0\n\t"
        "s_sub_i32 s64, s64, s66\n\t"
        "s_mul_i32 s93, s63, 18432\n\t"
        "s_mul_i32 s66, s64, 18432\n\t"
        "s_add_i32 s74, s91, 181248\n\t"
        "s_mul_i32 s67, s76, 18432\n\t"
        "s_sub_i32 s94, s74, s67\n\t"
        "s_add_i32 s94, s94, s93\n\t"
        "s_add_i32 s74, s74, s66\n\t"
        "s_cmp_ge_u32 s74, s94\n\t"
        "s_cselect_b32 s66, s93, 0\n\t"
        "s_sub_i32 s74, s74, s66\n\t"
        "s_mov_b32 s92, s64\n\t"
        "s_mul_i32 s78, s62, 9216\n\t"
        "v_lshrrev_b32 v105, 3, v104\n\t"
        "v_lshlrev_b32 v130, 11, v105\n\t"
        "v_and_b32 v105, 7, v104\n\t"
        "v_lshl_add_u32 v105, v3, 4, v105\n\t"
        "v_lshl_add_u32 v130, v105, 4, v130\n\t"
        "s_lshl_b32 s63, s76, 13\n\t"
        "s_lshl_b32 s64, s62, 12\n\t"
        "s_add_i32 s63, s63, s64\n\t"
        "s_add_i32 s63, s63, 61440\n\t"
        "v_add_u32 v130, s63, v130\n\t"
        "v_lshlrev_b32 v131, 3, v104\n\t"
        "v_add_u32 v131, s65, v131\n\t"
        "s_lshl_b32 s63, s62, 3\n\t"
        "v_add_u32 v105, s63, v3\n\t"
        "v_mul_u32_u24 v134, 9216, v105\n\t"
        "v_xor_b32 v106, v3, v104\n\t"
        "v_lshl_add_u32 v107, v106, 4, v134\n\t"
        "v_xor_b32 v106, 4, v106\n\t"
        "v_lshl_add_u32 v137, v106, 4, v134\n\t"
        "v_add_u32 v137, 36864, v137\n\t"
        "v_mov_b32 v134, v107\n\t"
        "s_lshl_b32 s63, s62, 11\n\t"
        "s_add_i32 s63, s63, 92160\n\t"
        "v_lshlrev_b32 v135, 4, v0\n\t"
        "v_add_u32 v135, s63, v135\n\t"
        "s_lshl_b32 s63, s62, 3\n\t"
        "v_add_u32 v135, s63, v135\n\t"
        "v_xor_b32 v136, 8, v135\n\t"
        "s_and_b32 s53, s13, 0xffff\n\t"
        "s_mov_b32 s52, s12\n\t"
        "s_mov_b32 s54, 18874368\n\t"
        "s_mov_b32 s55, 0x20000\n\t"
        "s_mul_i32 s77, s59, 147456\n\t"
        "s_lshl_b32 s63, s57, 8\n\t"
        "s_add_i32 s77, s77, s63\n\t"
        "s_sub_i32 s63, s60, 10\n\t"
        "s_mul_i32 s96, s63, 147456\n\t"
        "s_add_i32 s95, s77, 1474560\n\t"
        "s_mul_i32 s92, s92, 147456\n\t"
        "s_add_i32 s92, s92, s95\n\t"
        "s_add_i32 s95, s95, s96\n\t"
        "s_cmp_eq_u32 s60, 19\n\t"
        "s_cselect_b32 s79, 0, 1\n\t"
        "s_or_b32 s79, s79, s76\n\t"
        "s_lshl_b32 s79, s79, 30\n\t"
        "s_mul_i32 s72, s17, 9216\n\t"
        "s_add_i32 s72, s72, s90\n\t"
        "buffer_load_dwordx4 v[36:39], v112, s[40:43], s72 offen \n\t"
        "buffer_load_dwordx4 v[40:43], v113, s[40:43], s72 offen \n\t"
        "buffer_load_dwordx4 v[44:47], v114, s[40:43], s72 offen \n\t"
        "s_waitcnt vmcnt(8)\n\t"
        "v_lshlrev_b32 v132, 5, v104\n\t"
        "s_mul_i32 s63, s56, 4608\n\t"
        "s_add_i32 s63, s63, 103424\n\t"
        "v_add_u32 v132, s63, v132\n\t"
        "v_lshl_add_u32 v133, v3, 7, v131\n\t"
        "v_lshl_add_u32 v68, v3, 9, v132\n\t"
        "ds_read_b64 v[200:201], v133 offset:0\n\t"
        "ds_read_b64 v[202:203], v133 offset:1152\n\t"
        "ds_read_b64 v[204:205], v133 offset:512\n\t"
        "ds_read_b64 v[206:207], v133 offset:1664\n\t"
        "ds_read_b64 v[208:209], v133 offset:1024\n\t"
        "ds_read_b64 v[210:211], v133 offset:2176\n\t"
        "buffer_load_dwordx4 v[48:51], v115, s[40:43], s72 offen \n\t"
        "buffer_load_dwordx4 v[52:55], v116, s[40:43], s72 offen \n\t"
        "buffer_load_dwordx4 v[56:59], v117, s[40:43], s72 offen \n\t"
        "s_waitcnt lgkmcnt(4)\n\t"
        "v_lshlrev_b32 v212, 16, v200\n\t"
        "v_and_b32 v213, 0xffff0000, v200\n\t"
        "v_lshlrev_b32 v216, 16, v202\n\t"
        "v_and_b32 v217, 0xffff0000, v202\n\t"
        "v_lshlrev_b32 v214, 16, v201\n\t"
        "v_and_b32 v215, 0xffff0000, v201\n\t"
        "v_lshlrev_b32 v218, 16, v203\n\t"
        "v_and_b32 v219, 0xffff0000, v203\n\t"
        "v_mul_f32 v84, 0x4b800000, v212\n\t"
        "v_mul_f32_e64 v88, -v216, v212\n\t"
        "v_mul_f32 v85, 0x4b800000, v213\n\t"
        "v_mul_f32_e64 v89, -v217, v213\n\t"
        "v_mul_f32 v86, 0x4b800000, v214\n\t"
        "v_mul_f32_e64 v90, -v218, v214\n\t"
        "v_mul_f32 v87, 0x4b800000, v215\n\t"
        "v_mul_f32_e64 v91, -v219, v215\n\t"
        "ds_write_b128 v68, v[84:87] offset:0\n\t"
        "ds_write_b128 v68, v[88:91] offset:16\n\t"
        "s_waitcnt lgkmcnt(4)\n\t"
        "v_lshlrev_b32 v212, 16, v204\n\t"
        "v_and_b32 v213, 0xffff0000, v204\n\t"
        "v_lshlrev_b32 v216, 16, v206\n\t"
        "v_and_b32 v217, 0xffff0000, v206\n\t"
        "v_lshlrev_b32 v214, 16, v205\n\t"
        "v_and_b32 v215, 0xffff0000, v205\n\t"
        "v_lshlrev_b32 v218, 16, v207\n\t"
        "v_and_b32 v219, 0xffff0000, v207\n\t"
        "v_mul_f32 v92, 0x4b800000, v212\n\t"
        "v_mul_f32_e64 v96, -v216, v212\n\t"
        "v_mul_f32 v93, 0x4b800000, v213\n\t"
        "v_mul_f32_e64 v97, -v217, v213\n\t"
        "v_mul_f32 v94, 0x4b800000, v214\n\t"
        "v_mul_f32_e64 v98, -v218, v214\n\t"
        "v_mul_f32 v95, 0x4b800000, v215\n\t"
        "v_mul_f32_e64 v99, -v219, v215\n\t"
        "ds_write_b128 v68, v[92:95] offset:2048\n\t"
        "ds_write_b128 v68, v[96:99] offset:2064\n\t"
        "s_waitcnt lgkmcnt(4)\n\t"
        "v_lshlrev_b32 v212, 16, v208\n\t"
        "v_and_b32 v213, 0xffff0000, v208\n\t"
        "v_lshlrev_b32 v216, 16, v210\n\t"
        "v_and_b32 v217, 0xffff0000, v210\n\t"
        "v_lshlrev_b32 v214, 16, v209\n\t"
        "v_and_b32 v215, 0xffff0000, v209\n\t"
        "v_lshlrev_b32 v218, 16, v211\n\t"
        "v_and_b32 v219, 0xffff0000, v211\n\t"
        "v_mul_f32 v84, 0x4b800000, v212\n\t"
        "v_mul_f32_e64 v88, -v216, v212\n\t"
        "v_mul_f32 v85, 0x4b800000, v213\n\t"
        "v_mul_f32_e64 v89, -v217, v213\n\t"
        "v_mul_f32 v86, 0x4b800000, v214\n\t"
        "v_mul_f32_e64 v90, -v218, v214\n\t"
        "v_mul_f32 v87, 0x4b800000, v215\n\t"
        "v_mul_f32_e64 v91, -v219, v215\n\t"
        "s_mov_b64 exec, 0xffff\n\t"
        "ds_write_b128 v68, v[84:87] offset:4096\n\t"
        "ds_write_b128 v68, v[88:91] offset:4112\n\t"
        "s_mov_b64 exec, -1\n\t"
        "v_mad_u32_u24 v133, s16, 4, v132\n\t"
        "ds_read_b128 v[84:87], v133\n\t"
        "ds_read_b128 v[88:91], v133 offset:16\n\t"
        "s_waitcnt lgkmcnt(0)\n\t"
        "buffer_load_dwordx4 v[60:63], v118, s[40:43], s72 offen \n\t"
        "buffer_load_dwordx4 v[64:67], v119, s[40:43], s72 offen \n\t"
        "v_fma_mix_f32 v4, v4, v84, v88 op_sel_hi:[1,0,0]\n\t"
        "v_fma_mix_f32 v5, v5, v85, v89 op_sel_hi:[1,0,0]\n\t"
        "v_fma_mix_f32 v6, v6, v86, v90 op_sel_hi:[1,0,0]\n\t"
        "v_mad_u32_u24 v133, s17, 4, v132\n\t"
        "ds_read_b128 v[92:95], v133\n\t"
        "ds_read_b128 v[96:99], v133 offset:16\n\t"
        "v_fma_mix_f32 v7, v7, v87, v91 op_sel_hi:[1,0,0]\n\t"
        "v_fma_mix_f32 v8, v8, v84, v88 op_sel_hi:[1,0,0]\n\t"
        "v_fma_mix_f32 v9, v9, v85, v89 op_sel_hi:[1,0,0]\n\t"
        "v_fma_mix_f32 v10, v10, v86, v90 op_sel_hi:[1,0,0]\n\t"
        "v_fma_mix_f32 v11, v11, v87, v91 op_sel_hi:[1,0,0]\n\t"
        "v_cvt_pk_bf16_f32 v68, v4, v8\n\t"
        "v_cvt_pk_bf16_f32 v72, v5, v9\n\t"
        "v_cvt_pk_bf16_f32 v76, v6, v10\n\t"
        "v_cvt_pk_bf16_f32 v80, v7, v11\n\t"
        "v_fma_mix_f32 v12, v12, v84, v88 op_sel_hi:[1,0,0]\n\t"
        "v_fma_mix_f32 v13, v13, v85, v89 op_sel_hi:[1,0,0]\n\t"
        "v_fma_mix_f32 v14, v14, v86, v90 op_sel_hi:[1,0,0]\n\t"
        "v_fma_mix_f32 v15, v15, v87, v91 op_sel_hi:[1,0,0]\n\t"
        "v_fma_mix_f32 v16, v16, v84, v88 op_sel_hi:[1,0,0]\n\t"
        "v_fma_mix_f32 v17, v17, v85, v89 op_sel_hi:[1,0,0]\n\t"
        "v_fma_mix_f32 v18, v18, v86, v90 op_sel_hi:[1,0,0]\n\t"
        "v_fma_mix_f32 v19, v19, v87, v91 op_sel_hi:[1,0,0]\n\t"
        "v_cvt_pk_bf16_f32 v69, v12, v16\n\t"
        "v_cvt_pk_bf16_f32 v73, v13, v17\n\t"
        "v_cvt_pk_bf16_f32 v77, v14, v18\n\t"
        "v_cvt_pk_bf16_f32 v81, v15, v19\n\t"
        "v_fma_mix_f32 v20, v20, v84, v88 op_sel_hi:[1,0,0]\n\t"
        "v_fma_mix_f32 v21, v21, v85, v89 op_sel_hi:[1,0,0]\n\t"
        "v_fma_mix_f32 v22, v22, v86, v90 op_sel_hi:[1,0,0]\n\t"
        "v_fma_mix_f32 v23, v23, v87, v91 op_sel_hi:[1,0,0]\n\t"
        "v_fma_mix_f32 v24, v24, v84, v88 op_sel_hi:[1,0,0]\n\t"
        "v_fma_mix_f32 v25, v25, v85, v89 op_sel_hi:[1,0,0]\n\t"
        "v_fma_mix_f32 v26, v26, v86, v90 op_sel_hi:[1,0,0]\n\t"
        "v_fma_mix_f32 v27, v27, v87, v91 op_sel_hi:[1,0,0]\n\t"
        "v_cvt_pk_bf16_f32 v70, v20, v24\n\t"
        "v_cvt_pk_bf16_f32 v74, v21, v25\n\t"
        "v_cvt_pk_bf16_f32 v78, v22, v26\n\t"
        "v_cvt_pk_bf16_f32 v82, v23, v27\n\t"
        "v_fma_mix_f32 v28, v28, v84, v88 op_sel_hi:[1,0,0]\n\t"
        "v_fma_mix_f32 v29, v29, v85, v89 op_sel_hi:[1,0,0]\n\t"
        "v_fma_mix_f32 v30, v30, v86, v90 op_sel_hi:[1,0,0]\n\t"
        "v_fma_mix_f32 v31, v31, v87, v91 op_sel_hi:[1,0,0]\n\t"
        "v_fma_mix_f32 v32, v32, v84, v88 op_sel_hi:[1,0,0]\n\t"
        "v_fma_mix_f32 v33, v33, v85, v89 op_sel_hi:[1,0,0]\n\t"
        "v_fma_mix_f32 v34, v34, v86, v90 op_sel_hi:[1,0,0]\n\t"
        "v_fma_mix_f32 v35, v35, v87, v91 op_sel_hi:[1,0,0]\n\t"
        "v_cvt_pk_bf16_f32 v71, v28, v32\n\t"
        "v_cvt_pk_bf16_f32 v75, v29, v33\n\t"
        "v_cvt_pk_bf16_f32 v79, v30, v34\n\t"
        "v_cvt_pk_bf16_f32 v83, v31, v35\n\t"
        "ds_write_b128 v130, v[68:71] offset:0\n\t"
        "ds_write_b128 v130, v[72:75] offset:1024\n\t"
        "ds_write_b128 v130, v[76:79] offset:128\n\t"
        "ds_write_b128 v130, v[80:83] offset:1152\n\t"
        "s_add_i32 m0, s75, 20480\n\t"
        "s_add_i32 s73, s91, s17\n\t"
        "buffer_load_dwordx4 v120, s[36:39], s73 offen  lds\n\t"
        "buffer_load_dwordx4 v121, s[36:39], s73 offen offset:1024  lds\n\t"
        "buffer_load_dwordx4 v122, s[36:39], s73 offen offset:2048  lds\n\t"
        "buffer_load_dwordx4 v123, s[36:39], s73 offen offset:3072  lds\n\t"
        "s_add_i32 m0, s75, 24576\n\t"
        "s_nop 0\n\t"
        "buffer_load_dwordx4 v124, s[36:39], s73 offen  lds\n\t"
        "s_waitcnt vmcnt(13)\n\t"
        "s_waitcnt lgkmcnt(0)\n\t"
        "s_barrier\n\t"
        "L_pit0_%=:\n\t"
        "s_mul_i32 s72, s18, 9216\n\t"
        "s_add_i32 s72, s72, s90\n\t"
        "buffer_load_dwordx4 v[4:7], v112, s[40:43], s72 offen \n\t"
        "buffer_load_dwordx4 v[8:11], v113, s[40:43], s72 offen \n\t"
        "buffer_load_dwordx4 v[12:15], v114, s[40:43], s72 offen \n\t"
        "buffer_load_dwordx4 v[16:19], v115, s[40:43], s72 offen \n\t"
        "s_waitcnt vmcnt(15)\n\t"
        "v_fma_mix_f32 v36, v36, v92, v96 op_sel_hi:[1,0,0]\n\t"
        "v_fma_mix_f32 v37, v37, v93, v97 op_sel_hi:[1,0,0]\n\t"
        "v_fma_mix_f32 v38, v38, v94, v98 op_sel_hi:[1,0,0]\n\t"
        "v_mad_u32_u24 v133, s18, 4, v132\n\t"
        "ds_read_b128 v[84:87], v133\n\t"
        "ds_read_b128 v[88:91], v133 offset:16\n\t"
        "v_fma_mix_f32 v39, v39, v95, v99 op_sel_hi:[1,0,0]\n\t"
        "v_fma_mix_f32 v40, v40, v92, v96 op_sel_hi:[1,0,0]\n\t"
        "v_fma_mix_f32 v41, v41, v93, v97 op_sel_hi:[1,0,0]\n\t"
        "v_fma_mix_f32 v42, v42, v94, v98 op_sel_hi:[1,0,0]\n\t"
        "v_fma_mix_f32 v43, v43, v95, v99 op_sel_hi:[1,0,0]\n\t"
        "v_cvt_pk_bf16_f32 v68, v36, v40\n\t"
        "v_cvt_pk_bf16_f32 v72, v37, v41\n\t"
        "v_cvt_pk_bf16_f32 v76, v38, v42\n\t"
        "v_cvt_pk_bf16_f32 v80, v39, v43\n\t"
        "s_waitcnt vmcnt(13)\n\t"
        "v_fma_mix_f32 v44, v44, v92, v96 op_sel_hi:[1,0,0]\n\t"
        "buffer_load_dwordx4 v[20:23], v116, s[40:43], s72 offen \n\t"
        "v_fma_mix_f32 v45, v45, v93, v97 op_sel_hi:[1,0,0]\n\t"
        "v_fma_mix_f32 v46, v46, v94, v98 op_sel_hi:[1,0,0]\n\t"
        "v_fma_mix_f32 v47, v47, v95, v99 op_sel_hi:[1,0,0]\n\t"
        "v_fma_mix_f32 v48, v48, v92, v96 op_sel_hi:[1,0,0]\n\t"
        "v_fma_mix_f32 v49, v49, v93, v97 op_sel_hi:[1,0,0]\n\t"
        "v_fma_mix_f32 v50, v50, v94, v98 op_sel_hi:[1,0,0]\n\t"
        "v_fma_mix_f32 v51, v51, v95, v99 op_sel_hi:[1,0,0]\n\t"
        "v_cvt_pk_bf16_f32 v69, v44, v48\n\t"
        "v_cvt_pk_bf16_f32 v73, v45, v49\n\t"
        "v_cvt_pk_bf16_f32 v77, v46, v50\n\t"
        "v_cvt_pk_bf16_f32 v81, v47, v51\n\t"
        "s_waitcnt vmcnt(12)\n\t"
        "v_fma_mix_f32 v52, v52, v92, v96 op_sel_hi:[1,0,0]\n\t"
        "v_fma_mix_f32 v53, v53, v93, v97 op_sel_hi:[1,0,0]\n\t"
        "v_fma_mix_f32 v54, v54, v94, v98 op_sel_hi:[1,0,0]\n\t"
        "v_fma_mix_f32 v55, v55, v95, v99 op_sel_hi:[1,0,0]\n\t"
        "buffer_load_dwordx4 v[24:27], v117, s[40:43], s72 offen \n\t"
        "v_fma_mix_f32 v56, v56, v92, v96 op_sel_hi:[1,0,0]\n\t"
        "v_fma_mix_f32 v57, v57, v93, v97 op_sel_hi:[1,0,0]\n\t"
        "v_fma_mix_f32 v58, v58, v94, v98 op_sel_hi:[1,0,0]\n\t"
        "v_fma_mix_f32 v59, v59, v95, v99 op_sel_hi:[1,0,0]\n\t"
        "v_cvt_pk_bf16_f32 v70, v52, v56\n\t"
        "v_cvt_pk_bf16_f32 v74, v53, v57\n\t"
        "v_cvt_pk_bf16_f32 v78, v54, v58\n\t"
        "v_cvt_pk_bf16_f32 v82, v55, v59\n\t"
        "s_waitcnt vmcnt(11)\n\t"
        "v_fma_mix_f32 v60, v60, v92, v96 op_sel_hi:[1,0,0]\n\t"
        "v_fma_mix_f32 v61, v61, v93, v97 op_sel_hi:[1,0,0]\n\t"
        "v_fma_mix_f32 v62, v62, v94, v98 op_sel_hi:[1,0,0]\n\t"
        "v_fma_mix_f32 v63, v63, v95, v99 op_sel_hi:[1,0,0]\n\t"
        "v_fma_mix_f32 v64, v64, v92, v96 op_sel_hi:[1,0,0]\n\t"
        "v_fma_mix_f32 v65, v65, v93, v97 op_sel_hi:[1,0,0]\n\t"
        "v_fma_mix_f32 v66, v66, v94, v98 op_sel_hi:[1,0,0]\n\t"
        "buffer_load_dwordx4 v[28:31], v118, s[40:43], s72 offen \n\t"
        "v_fma_mix_f32 v67, v67, v95, v99 op_sel_hi:[1,0,0]\n\t"
        "v_cvt_pk_bf16_f32 v71, v60, v64\n\t"
        "v_cvt_pk_bf16_f32 v75, v61, v65\n\t"
        "v_cvt_pk_bf16_f32 v79, v62, v66\n\t"
        "v_cvt_pk_bf16_f32 v83, v63, v67\n\t"
        "ds_write_b128 v130, v[68:71] offset:16384\n\t"
        "ds_write_b128 v130, v[72:75] offset:17408\n\t"
        "ds_write_b128 v130, v[76:79] offset:16512\n\t"
        "ds_write_b128 v130, v[80:83] offset:17536\n\t"
        "buffer_load_dwordx4 v[32:35], v119, s[40:43], s72 offen \n\t"
        "s_add_i32 m0, s75, 40960\n\t"
        "s_add_i32 s73, s91, s18\n\t"
        "buffer_load_dwordx4 v120, s[36:39], s73 offen  lds\n\t"
        "buffer_load_dwordx4 v121, s[36:39], s73 offen offset:1024  lds\n\t"
        "buffer_load_dwordx4 v122, s[36:39], s73 offen offset:2048  lds\n\t"
        "buffer_load_dwordx4 v123, s[36:39], s73 offen offset:3072  lds\n\t"
        "s_add_i32 m0, s75, 45056\n\t"
        "s_nop 0\n\t"
        "buffer_load_dwordx4 v124, s[36:39], s73 offen  lds\n\t"
        "s_waitcnt vmcnt(13)\n\t"
        "s_waitcnt lgkmcnt(0)\n\t"
        "s_barrier\n\t"
        "L_pit1_%=:\n\t"
        "s_mul_i32 s72, s19, 9216\n\t"
        "s_add_i32 s72, s72, s90\n\t"
        "buffer_load_dwordx4 v[36:39], v112, s[40:43], s72 offen \n\t"
        "buffer_load_dwordx4 v[40:43], v113, s[40:43], s72 offen \n\t"
        "buffer_load_dwordx4 v[44:47], v114, s[40:43], s72 offen \n\t"
        "buffer_load_dwordx4 v[48:51], v115, s[40:43], s72 offen \n\t"
        "s_waitcnt vmcnt(15)\n\t"
        "v_fma_mix_f32 v4, v4, v84, v88 op_sel_hi:[1,0,0]\n\t"
        "v_fma_mix_f32 v5, v5, v85, v89 op_sel_hi:[1,0,0]\n\t"
        "v_fma_mix_f32 v6, v6, v86, v90 op_sel_hi:[1,0,0]\n\t"
        "v_mad_u32_u24 v133, s19, 4, v132\n\t"
        "ds_read_b128 v[92:95], v133\n\t"
        "ds_read_b128 v[96:99], v133 offset:16\n\t"
        "v_fma_mix_f32 v7, v7, v87, v91 op_sel_hi:[1,0,0]\n\t"
        "v_fma_mix_f32 v8, v8, v84, v88 op_sel_hi:[1,0,0]\n\t"
        "v_fma_mix_f32 v9, v9, v85, v89 op_sel_hi:[1,0,0]\n\t"
        "v_fma_mix_f32 v10, v10, v86, v90 op_sel_hi:[1,0,0]\n\t"
        "v_fma_mix_f32 v11, v11, v87, v91 op_sel_hi:[1,0,0]\n\t"
        "v_cvt_pk_bf16_f32 v68, v4, v8\n\t"
        "v_cvt_pk_bf16_f32 v72, v5, v9\n\t"
        "v_cvt_pk_bf16_f32 v76, v6, v10\n\t"
        "v_cvt_pk_bf16_f32 v80, v7, v11\n\t"
        "s_waitcnt vmcnt(13)\n\t"
        "v_fma_mix_f32 v12, v12, v84, v88 op_sel_hi:[1,0,0]\n\t"
        "buffer_load_dwordx4 v[52:55], v116, s[40:43], s72 offen \n\t"
        "v_fma_mix_f32 v13, v13, v85, v89 op_sel_hi:[1,0,0]\n\t"
        "v_fma_mix_f32 v14, v14, v86, v90 op_sel_hi:[1,0,0]\n\t"
        "v_fma_mix_f32 v15, v15, v87, v91 op_sel_hi:[1,0,0]\n\t"
        "v_fma_mix_f32 v16, v16, v84, v88 op_sel_hi:[1,0,0]\n\t"
        "v_fma_mix_f32 v17, v17, v85, v89 op_sel_hi:[1,0,0]\n\t"
        "v_fma_mix_f32 v18, v18, v86, v90 op_sel_hi:[1,0,0]\n\t"
        "v_fma_mix_f32 v19, v19, v87, v91 op_sel_hi:[1,0,0]\n\t"
        "v_cvt_pk_bf16_f32 v69, v12, v16\n\t"
        "v_cvt_pk_bf16_f32 v73, v13, v17\n\t"
        "v_cvt_pk_bf16_f32 v77, v14, v18\n\t"
        "v_cvt_pk_bf16_f32 v81, v15, v19\n\t"
        "s_waitcnt vmcnt(12)\n\t"
        "v_fma_mix_f32 v20, v20, v84, v88 op_sel_hi:[1,0,0]\n\t"
        "v_fma_mix_f32 v21, v21, v85, v89 op_sel_hi:[1,0,0]\n\t"
        "v_fma_mix_f32 v22, v22, v86, v90 op_sel_hi:[1,0,0]\n\t"
        "v_fma_mix_f32 v23, v23, v87, v91 op_sel_hi:[1,0,0]\n\t"
        "buffer_load_dwordx4 v[56:59], v117, s[40:43], s72 offen \n\t"
        "v_fma_mix_f32 v24, v24, v84, v88 op_sel_hi:[1,0,0]\n\t"
        "v_fma_mix_f32 v25, v25, v85, v89 op_sel_hi:[1,0,0]\n\t"
        "v_fma_mix_f32 v26, v26, v86, v90 op_sel_hi:[1,0,0]\n\t"
        "v_fma_mix_f32 v27, v27, v87, v91 op_sel_hi:[1,0,0]\n\t"
        "v_cvt_pk_bf16_f32 v70, v20, v24\n\t"
        "v_cvt_pk_bf16_f32 v74, v21, v25\n\t"
        "v_cvt_pk_bf16_f32 v78, v22, v26\n\t"
        "v_cvt_pk_bf16_f32 v82, v23, v27\n\t"
        "s_waitcnt vmcnt(11)\n\t"
        "v_fma_mix_f32 v28, v28, v84, v88 op_sel_hi:[1,0,0]\n\t"
        "v_fma_mix_f32 v29, v29, v85, v89 op_sel_hi:[1,0,0]\n\t"
        "v_fma_mix_f32 v30, v30, v86, v90 op_sel_hi:[1,0,0]\n\t"
        "v_fma_mix_f32 v31, v31, v87, v91 op_sel_hi:[1,0,0]\n\t"
        "v_fma_mix_f32 v32, v32, v84, v88 op_sel_hi:[1,0,0]\n\t"
        "v_fma_mix_f32 v33, v33, v85, v89 op_sel_hi:[1,0,0]\n\t"
        "v_fma_mix_f32 v34, v34, v86, v90 op_sel_hi:[1,0,0]\n\t"
        "buffer_load_dwordx4 v[60:63], v118, s[40:43], s72 offen \n\t"
        "v_fma_mix_f32 v35, v35, v87, v91 op_sel_hi:[1,0,0]\n\t"
        "v_cvt_pk_bf16_f32 v71, v28, v32\n\t"
        "v_cvt_pk_bf16_f32 v75, v29, v33\n\t"
        "v_cvt_pk_bf16_f32 v79, v30, v34\n\t"
        "v_cvt_pk_bf16_f32 v83, v31, v35\n\t"
        "ds_write_b128 v130, v[68:71] offset:0\n\t"
        "ds_write_b128 v130, v[72:75] offset:1024\n\t"
        "ds_write_b128 v130, v[76:79] offset:128\n\t"
        "ds_write_b128 v130, v[80:83] offset:1152\n\t"
        "buffer_load_dwordx4 v[64:67], v119, s[40:43], s72 offen \n\t"
        "s_add_i32 m0, s75, 0\n\t"
        "s_add_i32 s73, s91, s19\n\t"
        "buffer_load_dwordx4 v120, s[36:39], s73 offen  lds\n\t"
        "buffer_load_dwordx4 v121, s[36:39], s73 offen offset:1024  lds\n\t"
        "buffer_load_dwordx4 v122, s[36:39], s73 offen offset:2048  lds\n\t"
        "buffer_load_dwordx4 v123, s[36:39], s73 offen offset:3072  lds\n\t"
        "s_add_i32 m0, s75, 4096\n\t"
        "s_nop 0\n\t"
        "buffer_load_dwordx4 v124, s[36:39], s73 offen  lds\n\t"
        "s_waitcnt vmcnt(13)\n\t"
        "s_waitcnt lgkmcnt(0)\n\t"
        "s_barrier\n\t"
        "L_pit2_%=:\n\t"
        "s_mul_i32 s72, s20, 9216\n\t"
        "s_add_i32 s72, s72, s90\n\t"
        "buffer_load_dwordx4 v[4:7], v112, s[40:43], s72 offen \n\t"
        "buffer_load_dwordx4 v[8:11], v113, s[40:43], s72 offen \n\t"
        "buffer_load_dwordx4 v[12:15], v114, s[40:43], s72 offen \n\t"
        "buffer_load_dwordx4 v[16:19], v115, s[40:43], s72 offen \n\t"
        "s_waitcnt vmcnt(15)\n\t"
        "v_fma_mix_f32 v36, v36, v92, v96 op_sel_hi:[1,0,0]\n\t"
        "v_fma_mix_f32 v37, v37, v93, v97 op_sel_hi:[1,0,0]\n\t"
        "v_fma_mix_f32 v38, v38, v94, v98 op_sel_hi:[1,0,0]\n\t"
        "v_mad_u32_u24 v133, s20, 4, v132\n\t"
        "ds_read_b128 v[84:87], v133\n\t"
        "ds_read_b128 v[88:91], v133 offset:16\n\t"
        "v_fma_mix_f32 v39, v39, v95, v99 op_sel_hi:[1,0,0]\n\t"
        "v_fma_mix_f32 v40, v40, v92, v96 op_sel_hi:[1,0,0]\n\t"
        "v_fma_mix_f32 v41, v41, v93, v97 op_sel_hi:[1,0,0]\n\t"
        "v_fma_mix_f32 v42, v42, v94, v98 op_sel_hi:[1,0,0]\n\t"
        "v_fma_mix_f32 v43, v43, v95, v99 op_sel_hi:[1,0,0]\n\t"
        "v_cvt_pk_bf16_f32 v68, v36, v40\n\t"
        "v_cvt_pk_bf16_f32 v72, v37, v41\n\t"
        "v_cvt_pk_bf16_f32 v76, v38, v42\n\t"
        "v_cvt_pk_bf16_f32 v80, v39, v43\n\t"
        "s_waitcnt vmcnt(13)\n\t"
        "v_fma_mix_f32 v44, v44, v92, v96 op_sel_hi:[1,0,0]\n\t"
        "buffer_load_dwordx4 v[20:23], v116, s[40:43], s72 offen \n\t"
        "v_fma_mix_f32 v45, v45, v93, v97 op_sel_hi:[1,0,0]\n\t"
        "v_fma_mix_f32 v46, v46, v94, v98 op_sel_hi:[1,0,0]\n\t"
        "v_fma_mix_f32 v47, v47, v95, v99 op_sel_hi:[1,0,0]\n\t"
        "v_fma_mix_f32 v48, v48, v92, v96 op_sel_hi:[1,0,0]\n\t"
        "v_fma_mix_f32 v49, v49, v93, v97 op_sel_hi:[1,0,0]\n\t"
        "v_fma_mix_f32 v50, v50, v94, v98 op_sel_hi:[1,0,0]\n\t"
        "v_fma_mix_f32 v51, v51, v95, v99 op_sel_hi:[1,0,0]\n\t"
        "v_cvt_pk_bf16_f32 v69, v44, v48\n\t"
        "v_cvt_pk_bf16_f32 v73, v45, v49\n\t"
        "v_cvt_pk_bf16_f32 v77, v46, v50\n\t"
        "v_cvt_pk_bf16_f32 v81, v47, v51\n\t"
        "s_waitcnt vmcnt(12)\n\t"
        "v_fma_mix_f32 v52, v52, v92, v96 op_sel_hi:[1,0,0]\n\t"
        "v_fma_mix_f32 v53, v53, v93, v97 op_sel_hi:[1,0,0]\n\t"
        "v_fma_mix_f32 v54, v54, v94, v98 op_sel_hi:[1,0,0]\n\t"
        "v_fma_mix_f32 v55, v55, v95, v99 op_sel_hi:[1,0,0]\n\t"
        "buffer_load_dwordx4 v[24:27], v117, s[40:43], s72 offen \n\t"
        "v_fma_mix_f32 v56, v56, v92, v96 op_sel_hi:[1,0,0]\n\t"
        "v_fma_mix_f32 v57, v57, v93, v97 op_sel_hi:[1,0,0]\n\t"
        "v_fma_mix_f32 v58, v58, v94, v98 op_sel_hi:[1,0,0]\n\t"
        "v_fma_mix_f32 v59, v59, v95, v99 op_sel_hi:[1,0,0]\n\t"
        "v_cvt_pk_bf16_f32 v70, v52, v56\n\t"
        "v_cvt_pk_bf16_f32 v74, v53, v57\n\t"
        "v_cvt_pk_bf16_f32 v78, v54, v58\n\t"
        "v_cvt_pk_bf16_f32 v82, v55, v59\n\t"
        "s_waitcnt vmcnt(11)\n\t"
        "v_fma_mix_f32 v60, v60, v92, v96 op_sel_hi:[1,0,0]\n\t"
        "v_fma_mix_f32 v61, v61, v93, v97 op_sel_hi:[1,0,0]\n\t"
        "v_fma_mix_f32 v62, v62, v94, v98 op_sel_hi:[1,0,0]\n\t"
        "v_fma_mix_f32 v63, v63, v95, v99 op_sel_hi:[1,0,0]\n\t"
        "v_fma_mix_f32 v64, v64, v92, v96 op_sel_hi:[1,0,0]\n\t"
        "v_fma_mix_f32 v65, v65, v93, v97 op_sel_hi:[1,0,0]\n\t"
        "v_fma_mix_f32 v66, v66, v94, v98 op_sel_hi:[1,0,0]\n\t"
        "buffer_load_dwordx4 v[28:31], v118, s[40:43], s72 offen \n\t"
        "v_fma_mix_f32 v67, v67, v95, v99 op_sel_hi:[1,0,0]\n\t"
        "v_cvt_pk_bf16_f32 v71, v60, v64\n\t"
        "v_cvt_pk_bf16_f32 v75, v61, v65\n\t"
        "v_cvt_pk_bf16_f32 v79, v62, v66\n\t"
        "v_cvt_pk_bf16_f32 v83, v63, v67\n\t"
        "ds_write_b128 v130, v[68:71] offset:16384\n\t"
        "ds_write_b128 v130, v[72:75] offset:17408\n\t"
        "ds_write_b128 v130, v[76:79] offset:16512\n\t"
        "ds_write_b128 v130, v[80:83] offset:17536\n\t"
        "buffer_load_dwordx4 v[32:35], v119, s[40:43], s72 offen \n\t"
        "s_add_i32 m0, s75, 20480\n\t"
        "s_add_i32 s73, s91, s20\n\t"
        "buffer_load_dwordx4 v120, s[36:39], s73 offen  lds\n\t"
        "buffer_load_dwordx4 v121, s[36:39], s73 offen offset:1024  lds\n\t"
        "buffer_load_dwordx4 v122, s[36:39], s73 offen offset:2048  lds\n\t"
        "buffer_load_dwordx4 v123, s[36:39], s73 offen offset:3072  lds\n\t"
        "s_add_i32 m0, s75, 24576\n\t"
        "s_nop 0\n\t"
        "buffer_load_dwordx4 v124, s[36:39], s73 offen  lds\n\t"
        "s_waitcnt vmcnt(13)\n\t"
        "s_waitcnt lgkmcnt(0)\n\t"
        "s_barrier\n\t"
        "L_pit3_%=:\n\t"
        "s_mul_i32 s72, s21, 9216\n\t"
        "s_add_i32 s72, s72, s90\n\t"
        "buffer_load_dwordx4 v[36:39], v112, s[40:43], s72 offen \n\t"
        "buffer_load_dwordx4 v[40:43], v113, s[40:43], s72 offen \n\t"
        "buffer_load_dwordx4 v[44:47], v114, s[40:43], s72 offen \n\t"
        "buffer_load_dwordx4 v[48:51], v115, s[40:43], s72 offen \n\t"
        "s_waitcnt vmcnt(15)\n\t"
        "v_fma_mix_f32 v4, v4, v84, v88 op_sel_hi:[1,0,0]\n\t"
        "v_fma_mix_f32 v5, v5, v85, v89 op_sel_hi:[1,0,0]\n\t"
        "v_fma_mix_f32 v6, v6, v86, v90 op_sel_hi:[1,0,0]\n\t"
        "v_mad_u32_u24 v133, s21, 4, v132\n\t"
        "ds_read_b128 v[92:95], v133\n\t"
        "ds_read_b128 v[96:99], v133 offset:16\n\t"
        "v_fma_mix_f32 v7, v7, v87, v91 op_sel_hi:[1,0,0]\n\t"
        "v_fma_mix_f32 v8, v8, v84, v88 op_sel_hi:[1,0,0]\n\t"
        "v_fma_mix_f32 v9, v9, v85, v89 op_sel_hi:[1,0,0]\n\t"
        "v_fma_mix_f32 v10, v10, v86, v90 op_sel_hi:[1,0,0]\n\t"
        "v_fma_mix_f32 v11, v11, v87, v91 op_sel_hi:[1,0,0]\n\t"
        "v_cvt_pk_bf16_f32 v68, v4, v8\n\t"
        "v_cvt_pk_bf16_f32 v72, v5, v9\n\t"
        "v_cvt_pk_bf16_f32 v76, v6, v10\n\t"
        "v_cvt_pk_bf16_f32 v80, v7, v11\n\t"
        "s_waitcnt vmcnt(13)\n\t"
        "v_fma_mix_f32 v12, v12, v84, v88 op_sel_hi:[1,0,0]\n\t"
        "buffer_load_dwordx4 v[52:55], v116, s[40:43], s72 offen \n\t"
        "v_fma_mix_f32 v13, v13, v85, v89 op_sel_hi:[1,0,0]\n\t"
        "v_fma_mix_f32 v14, v14, v86, v90 op_sel_hi:[1,0,0]\n\t"
        "v_fma_mix_f32 v15, v15, v87, v91 op_sel_hi:[1,0,0]\n\t"
        "v_fma_mix_f32 v16, v16, v84, v88 op_sel_hi:[1,0,0]\n\t"
        "v_fma_mix_f32 v17, v17, v85, v89 op_sel_hi:[1,0,0]\n\t"
        "v_fma_mix_f32 v18, v18, v86, v90 op_sel_hi:[1,0,0]\n\t"
        "v_fma_mix_f32 v19, v19, v87, v91 op_sel_hi:[1,0,0]\n\t"
        "v_cvt_pk_bf16_f32 v69, v12, v16\n\t"
        "v_cvt_pk_bf16_f32 v73, v13, v17\n\t"
        "v_cvt_pk_bf16_f32 v77, v14, v18\n\t"
        "v_cvt_pk_bf16_f32 v81, v15, v19\n\t"
        "s_waitcnt vmcnt(12)\n\t"
        "v_fma_mix_f32 v20, v20, v84, v88 op_sel_hi:[1,0,0]\n\t"
        "v_fma_mix_f32 v21, v21, v85, v89 op_sel_hi:[1,0,0]\n\t"
        "v_fma_mix_f32 v22, v22, v86, v90 op_sel_hi:[1,0,0]\n\t"
        "v_fma_mix_f32 v23, v23, v87, v91 op_sel_hi:[1,0,0]\n\t"
        "buffer_load_dwordx4 v[56:59], v117, s[40:43], s72 offen \n\t"
        "v_fma_mix_f32 v24, v24, v84, v88 op_sel_hi:[1,0,0]\n\t"
        "v_fma_mix_f32 v25, v25, v85, v89 op_sel_hi:[1,0,0]\n\t"
        "v_fma_mix_f32 v26, v26, v86, v90 op_sel_hi:[1,0,0]\n\t"
        "v_fma_mix_f32 v27, v27, v87, v91 op_sel_hi:[1,0,0]\n\t"
        "v_cvt_pk_bf16_f32 v70, v20, v24\n\t"
        "v_cvt_pk_bf16_f32 v74, v21, v25\n\t"
        "v_cvt_pk_bf16_f32 v78, v22, v26\n\t"
        "v_cvt_pk_bf16_f32 v82, v23, v27\n\t"
        "s_waitcnt vmcnt(11)\n\t"
        "v_fma_mix_f32 v28, v28, v84, v88 op_sel_hi:[1,0,0]\n\t"
        "v_fma_mix_f32 v29, v29, v85, v89 op_sel_hi:[1,0,0]\n\t"
        "v_fma_mix_f32 v30, v30, v86, v90 op_sel_hi:[1,0,0]\n\t"
        "v_fma_mix_f32 v31, v31, v87, v91 op_sel_hi:[1,0,0]\n\t"
        "v_fma_mix_f32 v32, v32, v84, v88 op_sel_hi:[1,0,0]\n\t"
        "v_fma_mix_f32 v33, v33, v85, v89 op_sel_hi:[1,0,0]\n\t"
        "v_fma_mix_f32 v34, v34, v86, v90 op_sel_hi:[1,0,0]\n\t"
        "buffer_load_dwordx4 v[60:63], v118, s[40:43], s72 offen \n\t"
        "v_fma_mix_f32 v35, v35, v87, v91 op_sel_hi:[1,0,0]\n\t"
        "v_cvt_pk_bf16_f32 v71, v28, v32\n\t"
        "v_cvt_pk_bf16_f32 v75, v29, v33\n\t"
        "v_cvt_pk_bf16_f32 v79, v30, v34\n\t"
        "v_cvt_pk_bf16_f32 v83, v31, v35\n\t"
        "ds_write_b128 v130, v[68:71] offset:0\n\t"
        "ds_write_b128 v130, v[72:75] offset:1024\n\t"
        "ds_write_b128 v130, v[76:79] offset:128\n\t"
        "ds_write_b128 v130, v[80:83] offset:1152\n\t"
        "buffer_load_dwordx4 v[64:67], v119, s[40:43], s72 offen \n\t"
        "s_add_i32 m0, s75, 40960\n\t"
        "s_add_i32 s73, s91, s21\n\t"
        "buffer_load_dwordx4 v120, s[36:39], s73 offen  lds\n\t"
        "buffer_load_dwordx4 v121, s[36:39], s73 offen offset:1024  lds\n\t"
        "buffer_load_dwordx4 v122, s[36:39], s73 offen offset:2048  lds\n\t"
        "buffer_load_dwordx4 v123, s[36:39], s73 offen offset:3072  lds\n\t"
        "s_add_i32 m0, s75, 45056\n\t"
        "s_nop 0\n\t"
        "buffer_load_dwordx4 v124, s[36:39], s73 offen  lds\n\t"
        "s_waitcnt vmcnt(13)\n\t"
        "s_waitcnt lgkmcnt(0)\n\t"
        "s_barrier\n\t"
        "L_pit4_%=:\n\t"
        "s_mul_i32 s72, s22, 9216\n\t"
        "s_add_i32 s72, s72, s90\n\t"
        "buffer_load_dwordx4 v[4:7], v112, s[40:43], s72 offen \n\t"
        "buffer_load_dwordx4 v[8:11], v113, s[40:43], s72 offen \n\t"
        "buffer_load_dwordx4 v[12:15], v114, s[40:43], s72 offen \n\t"
        "buffer_load_dwordx4 v[16:19], v115, s[40:43], s72 offen \n\t"
        "s_waitcnt vmcnt(15)\n\t"
        "v_fma_mix_f32 v36, v36, v92, v96 op_sel_hi:[1,0,0]\n\t"
        "v_fma_mix_f32 v37, v37, v93, v97 op_sel_hi:[1,0,0]\n\t"
        "v_fma_mix_f32 v38, v38, v94, v98 op_sel_hi:[1,0,0]\n\t"
        "v_mad_u32_u24 v133, s22, 4, v132\n\t"
        "ds_read_b128 v[84:87], v133\n\t"
        "ds_read_b128 v[88:91], v133 offset:16\n\t"
        "v_fma_mix_f32 v39, v39, v95, v99 op_sel_hi:[1,0,0]\n\t"
        "v_fma_mix_f32 v40, v40, v92, v96 op_sel_hi:[1,0,0]\n\t"
        "v_fma_mix_f32 v41, v41, v93, v97 op_sel_hi:[1,0,0]\n\t"
        "v_fma_mix_f32 v42, v42, v94, v98 op_sel_hi:[1,0,0]\n\t"
        "v_fma_mix_f32 v43, v43, v95, v99 op_sel_hi:[1,0,0]\n\t"
        "v_cvt_pk_bf16_f32 v68, v36, v40\n\t"
        "v_cvt_pk_bf16_f32 v72, v37, v41\n\t"
        "v_cvt_pk_bf16_f32 v76, v38, v42\n\t"
        "v_cvt_pk_bf16_f32 v80, v39, v43\n\t"
        "s_waitcnt vmcnt(13)\n\t"
        "v_fma_mix_f32 v44, v44, v92, v96 op_sel_hi:[1,0,0]\n\t"
        "buffer_load_dwordx4 v[20:23], v116, s[40:43], s72 offen \n\t"
        "v_fma_mix_f32 v45, v45, v93, v97 op_sel_hi:[1,0,0]\n\t"
        "v_fma_mix_f32 v46, v46, v94, v98 op_sel_hi:[1,0,0]\n\t"
        "v_fma_mix_f32 v47, v47, v95, v99 op_sel_hi:[1,0,0]\n\t"
        "v_fma_mix_f32 v48, v48, v92, v96 op_sel_hi:[1,0,0]\n\t"
        "v_fma_mix_f32 v49, v49, v93, v97 op_sel_hi:[1,0,0]\n\t"
        "v_fma_mix_f32 v50, v50, v94, v98 op_sel_hi:[1,0,0]\n\t"
        "v_fma_mix_f32 v51, v51, v95, v99 op_sel_hi:[1,0,0]\n\t"
        "v_cvt_pk_bf16_f32 v69, v44, v48\n\t"
        "v_cvt_pk_bf16_f32 v73, v45, v49\n\t"
        "v_cvt_pk_bf16_f32 v77, v46, v50\n\t"
        "v_cvt_pk_bf16_f32 v81, v47, v51\n\t"
        "s_waitcnt vmcnt(12)\n\t"
        "v_fma_mix_f32 v52, v52, v92, v96 op_sel_hi:[1,0,0]\n\t"
        "v_fma_mix_f32 v53, v53, v93, v97 op_sel_hi:[1,0,0]\n\t"
        "v_fma_mix_f32 v54, v54, v94, v98 op_sel_hi:[1,0,0]\n\t"
        "v_fma_mix_f32 v55, v55, v95, v99 op_sel_hi:[1,0,0]\n\t"
        "buffer_load_dwordx4 v[24:27], v117, s[40:43], s72 offen \n\t"
        "v_fma_mix_f32 v56, v56, v92, v96 op_sel_hi:[1,0,0]\n\t"
        "v_fma_mix_f32 v57, v57, v93, v97 op_sel_hi:[1,0,0]\n\t"
        "v_fma_mix_f32 v58, v58, v94, v98 op_sel_hi:[1,0,0]\n\t"
        "v_fma_mix_f32 v59, v59, v95, v99 op_sel_hi:[1,0,0]\n\t"
        "v_cvt_pk_bf16_f32 v70, v52, v56\n\t"
        "v_cvt_pk_bf16_f32 v74, v53, v57\n\t"
        "v_cvt_pk_bf16_f32 v78, v54, v58\n\t"
        "v_cvt_pk_bf16_f32 v82, v55, v59\n\t"
        "s_waitcnt vmcnt(11)\n\t"
        "v_fma_mix_f32 v60, v60, v92, v96 op_sel_hi:[1,0,0]\n\t"
        "v_fma_mix_f32 v61, v61, v93, v97 op_sel_hi:[1,0,0]\n\t"
        "v_fma_mix_f32 v62, v62, v94, v98 op_sel_hi:[1,0,0]\n\t"
        "v_fma_mix_f32 v63, v63, v95, v99 op_sel_hi:[1,0,0]\n\t"
        "v_fma_mix_f32 v64, v64, v92, v96 op_sel_hi:[1,0,0]\n\t"
        "v_fma_mix_f32 v65, v65, v93, v97 op_sel_hi:[1,0,0]\n\t"
        "v_fma_mix_f32 v66, v66, v94, v98 op_sel_hi:[1,0,0]\n\t"
        "buffer_load_dwordx4 v[28:31], v118, s[40:43], s72 offen \n\t"
        "v_fma_mix_f32 v67, v67, v95, v99 op_sel_hi:[1,0,0]\n\t"
        "v_cvt_pk_bf16_f32 v71, v60, v64\n\t"
        "v_cvt_pk_bf16_f32 v75, v61, v65\n\t"
        "v_cvt_pk_bf16_f32 v79, v62, v66\n\t"
        "v_cvt_pk_bf16_f32 v83, v63, v67\n\t"
        "ds_write_b128 v130, v[68:71] offset:16384\n\t"
        "ds_write_b128 v130, v[72:75] offset:17408\n\t"
        "ds_write_b128 v130, v[76:79] offset:16512\n\t"
        "ds_write_b128 v130, v[80:83] offset:17536\n\t"
        "buffer_load_dwordx4 v[32:35], v119, s[40:43], s72 offen \n\t"
        "s_add_i32 m0, s75, 0\n\t"
        "s_add_i32 s73, s91, s22\n\t"
        "buffer_load_dwordx4 v120, s[36:39], s73 offen  lds\n\t"
        "buffer_load_dwordx4 v121, s[36:39], s73 offen offset:1024  lds\n\t"
        "buffer_load_dwordx4 v122, s[36:39], s73 offen offset:2048  lds\n\t"
        "buffer_load_dwordx4 v123, s[36:39], s73 offen offset:3072  lds\n\t"
        "s_add_i32 m0, s75, 4096\n\t"
        "s_nop 0\n\t"
        "buffer_load_dwordx4 v124, s[36:39], s73 offen  lds\n\t"
        "s_waitcnt vmcnt(13)\n\t"
        "s_waitcnt lgkmcnt(0)\n\t"
        "s_barrier\n\t"
        "L_pit5_%=:\n\t"
        "s_mul_i32 s72, s23, 9216\n\t"
        "s_add_i32 s72, s72, s90\n\t"
        "buffer_load_dwordx4 v[36:39], v112, s[40:43], s72 offen \n\t"
        "buffer_load_dwordx4 v[40:43], v113, s[40:43], s72 offen \n\t"
        "buffer_load_dwordx4 v[44:47], v114, s[40:43], s72 offen \n\t"
        "buffer_load_dwordx4 v[48:51], v115, s[40:43], s72 offen \n\t"
        "s_waitcnt vmcnt(15)\n\t"
        "v_fma_mix_f32 v4, v4, v84, v88 op_sel_hi:[1,0,0]\n\t"
        "v_fma_mix_f32 v5, v5, v85, v89 op_sel_hi:[1,0,0]\n\t"
        "v_fma_mix_f32 v6, v6, v86, v90 op_sel_hi:[1,0,0]\n\t"
        "v_mad_u32_u24 v133, s23, 4, v132\n\t"
        "ds_read_b128 v[92:95], v133\n\t"
        "ds_read_b128 v[96:99], v133 offset:16\n\t"
        "v_fma_mix_f32 v7, v7, v87, v91 op_sel_hi:[1,0,0]\n\t"
        "v_fma_mix_f32 v8, v8, v84, v88 op_sel_hi:[1,0,0]\n\t"
        "v_fma_mix_f32 v9, v9, v85, v89 op_sel_hi:[1,0,0]\n\t"
        "v_fma_mix_f32 v10, v10, v86, v90 op_sel_hi:[1,0,0]\n\t"
        "v_fma_mix_f32 v11, v11, v87, v91 op_sel_hi:[1,0,0]\n\t"
        "v_cvt_pk_bf16_f32 v68, v4, v8\n\t"
        "v_cvt_pk_bf16_f32 v72, v5, v9\n\t"
        "v_cvt_pk_bf16_f32 v76, v6, v10\n\t"
        "v_cvt_pk_bf16_f32 v80, v7, v11\n\t"
        "s_waitcnt vmcnt(13)\n\t"
        "v_fma_mix_f32 v12, v12, v84, v88 op_sel_hi:[1,0,0]\n\t"
        "buffer_load_dwordx4 v[52:55], v116, s[40:43], s72 offen \n\t"
        "v_fma_mix_f32 v13, v13, v85, v89 op_sel_hi:[1,0,0]\n\t"
        "v_fma_mix_f32 v14, v14, v86, v90 op_sel_hi:[1,0,0]\n\t"
        "v_fma_mix_f32 v15, v15, v87, v91 op_sel_hi:[1,0,0]\n\t"
        "v_fma_mix_f32 v16, v16, v84, v88 op_sel_hi:[1,0,0]\n\t"
        "v_fma_mix_f32 v17, v17, v85, v89 op_sel_hi:[1,0,0]\n\t"
        "v_fma_mix_f32 v18, v18, v86, v90 op_sel_hi:[1,0,0]\n\t"
        "v_fma_mix_f32 v19, v19, v87, v91 op_sel_hi:[1,0,0]\n\t"
        "v_cvt_pk_bf16_f32 v69, v12, v16\n\t"
        "v_cvt_pk_bf16_f32 v73, v13, v17\n\t"
        "v_cvt_pk_bf16_f32 v77, v14, v18\n\t"
        "v_cvt_pk_bf16_f32 v81, v15, v19\n\t"
        "s_waitcnt vmcnt(12)\n\t"
        "v_fma_mix_f32 v20, v20, v84, v88 op_sel_hi:[1,0,0]\n\t"
        "v_fma_mix_f32 v21, v21, v85, v89 op_sel_hi:[1,0,0]\n\t"
        "v_fma_mix_f32 v22, v22, v86, v90 op_sel_hi:[1,0,0]\n\t"
        "v_fma_mix_f32 v23, v23, v87, v91 op_sel_hi:[1,0,0]\n\t"
        "buffer_load_dwordx4 v[56:59], v117, s[40:43], s72 offen \n\t"
        "v_fma_mix_f32 v24, v24, v84, v88 op_sel_hi:[1,0,0]\n\t"
        "v_fma_mix_f32 v25, v25, v85, v89 op_sel_hi:[1,0,0]\n\t"
        "v_fma_mix_f32 v26, v26, v86, v90 op_sel_hi:[1,0,0]\n\t"
        "v_fma_mix_f32 v27, v27, v87, v91 op_sel_hi:[1,0,0]\n\t"
        "v_cvt_pk_bf16_f32 v70, v20, v24\n\t"
        "v_cvt_pk_bf16_f32 v74, v21, v25\n\t"
        "v_cvt_pk_bf16_f32 v78, v22, v26\n\t"
        "v_cvt_pk_bf16_f32 v82, v23, v27\n\t"
        "s_waitcnt vmcnt(11)\n\t"
        "v_fma_mix_f32 v28, v28, v84, v88 op_sel_hi:[1,0,0]\n\t"
        "v_fma_mix_f32 v29, v29, v85, v89 op_sel_hi:[1,0,0]\n\t"
        "v_fma_mix_f32 v30, v30, v86, v90 op_sel_hi:[1,0,0]\n\t"
        "v_fma_mix_f32 v31, v31, v87, v91 op_sel_hi:[1,0,0]\n\t"
        "v_fma_mix_f32 v32, v32, v84, v88 op_sel_hi:[1,0,0]\n\t"
        "v_fma_mix_f32 v33, v33, v85, v89 op_sel_hi:[1,0,0]\n\t"
        "v_fma_mix_f32 v34, v34, v86, v90 op_sel_hi:[1,0,0]\n\t"
        "buffer_load_dwordx4 v[60:63], v118, s[40:43], s72 offen \n\t"
        "v_fma_mix_f32 v35, v35, v87, v91 op_sel_hi:[1,0,0]\n\t"
        "v_cvt_pk_bf16_f32 v71, v28, v32\n\t"
        "v_cvt_pk_bf16_f32 v75, v29, v33\n\t"
        "v_cvt_pk_bf16_f32 v79, v30, v34\n\t"
        "v_cvt_pk_bf16_f32 v83, v31, v35\n\t"
        "ds_write_b128 v130, v[68:71] offset:0\n\t"
        "ds_write_b128 v130, v[72:75] offset:1024\n\t"
        "ds_write_b128 v130, v[76:79] offset:128\n\t"
        "ds_write_b128 v130, v[80:83] offset:1152\n\t"
        "buffer_load_dwordx4 v[64:67], v119, s[40:43], s72 offen \n\t"
        "s_add_i32 m0, s75, 20480\n\t"
        "s_add_i32 s73, s91, s23\n\t"
        "buffer_load_dwordx4 v120, s[36:39], s73 offen  lds\n\t"
        "buffer_load_dwordx4 v121, s[36:39], s73 offen offset:1024  lds\n\t"
        "buffer_load_dwordx4 v122, s[36:39], s73 offen offset:2048  lds\n\t"
        "buffer_load_dwordx4 v123, s[36:39], s73 offen offset:3072  lds\n\t"
        "s_add_i32 m0, s75, 24576\n\t"
        "s_nop 0\n\t"
        "buffer_load_dwordx4 v124, s[36:39], s73 offen  lds\n\t"
        "s_waitcnt vmcnt(13)\n\t"
        "s_waitcnt lgkmcnt(0)\n\t"
        "s_barrier\n\t"
        "L_pit6_%=:\n\t"
        "s_mul_i32 s72, s24, 9216\n\t"
        "s_add_i32 s72, s72, s90\n\t"
        "buffer_load_dwordx4 v[4:7], v112, s[40:43], s72 offen \n\t"
        "buffer_load_dwordx4 v[8:11], v113, s[40:43], s72 offen \n\t"
        "buffer_load_dwordx4 v[12:15], v114, s[40:43], s72 offen \n\t"
        "buffer_load_dwordx4 v[16:19], v115, s[40:43], s72 offen \n\t"
        "s_waitcnt vmcnt(15)\n\t"
        "v_fma_mix_f32 v36, v36, v92, v96 op_sel_hi:[1,0,0]\n\t"
        "v_fma_mix_f32 v37, v37, v93, v97 op_sel_hi:[1,0,0]\n\t"
        "v_fma_mix_f32 v38, v38, v94, v98 op_sel_hi:[1,0,0]\n\t"
        "v_mad_u32_u24 v133, s24, 4, v132\n\t"
        "ds_read_b128 v[84:87], v133\n\t"
        "ds_read_b128 v[88:91], v133 offset:16\n\t"
        "v_fma_mix_f32 v39, v39, v95, v99 op_sel_hi:[1,0,0]\n\t"
        "v_fma_mix_f32 v40, v40, v92, v96 op_sel_hi:[1,0,0]\n\t"
        "v_fma_mix_f32 v41, v41, v93, v97 op_sel_hi:[1,0,0]\n\t"
        "v_fma_mix_f32 v42, v42, v94, v98 op_sel_hi:[1,0,0]\n\t"
        "v_fma_mix_f32 v43, v43, v95, v99 op_sel_hi:[1,0,0]\n\t"
        "v_cvt_pk_bf16_f32 v68, v36, v40\n\t"
        "v_cvt_pk_bf16_f32 v72, v37, v41\n\t"
        "v_cvt_pk_bf16_f32 v76, v38, v42\n\t"
        "v_cvt_pk_bf16_f32 v80, v39, v43\n\t"
        "s_waitcnt vmcnt(13)\n\t"
        "v_fma_mix_f32 v44, v44, v92, v96 op_sel_hi:[1,0,0]\n\t"
        "buffer_load_dwordx4 v[20:23], v116, s[40:43], s72 offen \n\t"
        "v_fma_mix_f32 v45, v45, v93, v97 op_sel_hi:[1,0,0]\n\t"
        "v_fma_mix_f32 v46, v46, v94, v98 op_sel_hi:[1,0,0]\n\t"
        "v_fma_mix_f32 v47, v47, v95, v99 op_sel_hi:[1,0,0]\n\t"
        "v_fma_mix_f32 v48, v48, v92, v96 op_sel_hi:[1,0,0]\n\t"
        "v_fma_mix_f32 v49, v49, v93, v97 op_sel_hi:[1,0,0]\n\t"
        "v_fma_mix_f32 v50, v50, v94, v98 op_sel_hi:[1,0,0]\n\t"
        "v_fma_mix_f32 v51, v51, v95, v99 op_sel_hi:[1,0,0]\n\t"
        "v_cvt_pk_bf16_f32 v69, v44, v48\n\t"
        "v_cvt_pk_bf16_f32 v73, v45, v49\n\t"
        "v_cvt_pk_bf16_f32 v77, v46, v50\n\t"
        "v_cvt_pk_bf16_f32 v81, v47, v51\n\t"
        "s_waitcnt vmcnt(12)\n\t"
        "v_fma_mix_f32 v52, v52, v92, v96 op_sel_hi:[1,0,0]\n\t"
        "v_fma_mix_f32 v53, v53, v93, v97 op_sel_hi:[1,0,0]\n\t"
        "v_fma_mix_f32 v54, v54, v94, v98 op_sel_hi:[1,0,0]\n\t"
        "v_fma_mix_f32 v55, v55, v95, v99 op_sel_hi:[1,0,0]\n\t"
        "buffer_load_dwordx4 v[24:27], v117, s[40:43], s72 offen \n\t"
        "v_fma_mix_f32 v56, v56, v92, v96 op_sel_hi:[1,0,0]\n\t"
        "v_fma_mix_f32 v57, v57, v93, v97 op_sel_hi:[1,0,0]\n\t"
        "v_fma_mix_f32 v58, v58, v94, v98 op_sel_hi:[1,0,0]\n\t"
        "v_fma_mix_f32 v59, v59, v95, v99 op_sel_hi:[1,0,0]\n\t"
        "v_cvt_pk_bf16_f32 v70, v52, v56\n\t"
        "v_cvt_pk_bf16_f32 v74, v53, v57\n\t"
        "v_cvt_pk_bf16_f32 v78, v54, v58\n\t"
        "v_cvt_pk_bf16_f32 v82, v55, v59\n\t"
        "s_waitcnt vmcnt(11)\n\t"
        "v_fma_mix_f32 v60, v60, v92, v96 op_sel_hi:[1,0,0]\n\t"
        "v_fma_mix_f32 v61, v61, v93, v97 op_sel_hi:[1,0,0]\n\t"
        "v_fma_mix_f32 v62, v62, v94, v98 op_sel_hi:[1,0,0]\n\t"
        "v_fma_mix_f32 v63, v63, v95, v99 op_sel_hi:[1,0,0]\n\t"
        "v_fma_mix_f32 v64, v64, v92, v96 op_sel_hi:[1,0,0]\n\t"
        "v_fma_mix_f32 v65, v65, v93, v97 op_sel_hi:[1,0,0]\n\t"
        "v_fma_mix_f32 v66, v66, v94, v98 op_sel_hi:[1,0,0]\n\t"
        "buffer_load_dwordx4 v[28:31], v118, s[40:43], s72 offen \n\t"
        "v_fma_mix_f32 v67, v67, v95, v99 op_sel_hi:[1,0,0]\n\t"
        "v_cvt_pk_bf16_f32 v71, v60, v64\n\t"
        "v_cvt_pk_bf16_f32 v75, v61, v65\n\t"
        "v_cvt_pk_bf16_f32 v79, v62, v66\n\t"
        "v_cvt_pk_bf16_f32 v83, v63, v67\n\t"
        "ds_write_b128 v130, v[68:71] offset:16384\n\t"
        "ds_write_b128 v130, v[72:75] offset:17408\n\t"
        "ds_write_b128 v130, v[76:79] offset:16512\n\t"
        "ds_write_b128 v130, v[80:83] offset:17536\n\t"
        "buffer_load_dwordx4 v[32:35], v119, s[40:43], s72 offen \n\t"
        "s_add_i32 m0, s75, 40960\n\t"
        "s_add_i32 s73, s91, s24\n\t"
        "buffer_load_dwordx4 v120, s[36:39], s73 offen  lds\n\t"
        "buffer_load_dwordx4 v121, s[36:39], s73 offen offset:1024  lds\n\t"
        "buffer_load_dwordx4 v122, s[36:39], s73 offen offset:2048  lds\n\t"
        "buffer_load_dwordx4 v123, s[36:39], s73 offen offset:3072  lds\n\t"
        "s_add_i32 m0, s75, 45056\n\t"
        "s_nop 0\n\t"
        "buffer_load_dwordx4 v124, s[36:39], s73 offen  lds\n\t"
        "s_waitcnt vmcnt(13)\n\t"
        "s_waitcnt lgkmcnt(0)\n\t"
        "s_barrier\n\t"
        "L_pit7_%=:\n\t"
        "s_mul_i32 s68, s76, 18432\n\t"
        "s_add_i32 s68, s68, s78\n\t"
        "s_add_i32 s68, s68, 126976\n\t"
        "s_add_i32 m0, s68, 0\n\t"
        "s_add_i32 s69, s74, s16\n\t"
        "s_add_i32 s69, s69, 3072\n\t"
        "buffer_load_dwordx4 v120, s[36:39], s69 offen  lds\n\t"
        "s_add_i32 s69, s74, s17\n\t"
        "s_add_i32 s69, s69, 2048\n\t"
        "buffer_load_dwordx4 v120, s[36:39], s69 offen offset:1024  lds\n\t"
        "s_add_i32 s69, s74, s18\n\t"
        "s_add_i32 s69, s69, 1024\n\t"
        "buffer_load_dwordx4 v120, s[36:39], s69 offen offset:2048  lds\n\t"
        "s_add_i32 s69, s74, s19\n\t"
        "s_add_i32 s69, s69, 0\n\t"
        "buffer_load_dwordx4 v120, s[36:39], s69 offen offset:3072  lds\n\t"
        "s_waitcnt vmcnt(15)\n\t"
        "v_fma_mix_f32 v4, v4, v84, v88 op_sel_hi:[1,0,0]\n\t"
        "v_fma_mix_f32 v5, v5, v85, v89 op_sel_hi:[1,0,0]\n\t"
        "v_fma_mix_f32 v6, v6, v86, v90 op_sel_hi:[1,0,0]\n\t"
        "v_fma_mix_f32 v7, v7, v87, v91 op_sel_hi:[1,0,0]\n\t"
        "v_fma_mix_f32 v8, v8, v84, v88 op_sel_hi:[1,0,0]\n\t"
        "v_fma_mix_f32 v9, v9, v85, v89 op_sel_hi:[1,0,0]\n\t"
        "v_fma_mix_f32 v10, v10, v86, v90 op_sel_hi:[1,0,0]\n\t"
        "v_fma_mix_f32 v11, v11, v87, v91 op_sel_hi:[1,0,0]\n\t"
        "v_cvt_pk_bf16_f32 v68, v4, v8\n\t"
        "v_cvt_pk_bf16_f32 v72, v5, v9\n\t"
        "v_cvt_pk_bf16_f32 v76, v6, v10\n\t"
        "v_cvt_pk_bf16_f32 v80, v7, v11\n\t"
        "s_waitcnt vmcnt(13)\n\t"
        "v_fma_mix_f32 v12, v12, v84, v88 op_sel_hi:[1,0,0]\n\t"
        "v_fma_mix_f32 v13, v13, v85, v89 op_sel_hi:[1,0,0]\n\t"
        "v_fma_mix_f32 v14, v14, v86, v90 op_sel_hi:[1,0,0]\n\t"
        "v_fma_mix_f32 v15, v15, v87, v91 op_sel_hi:[1,0,0]\n\t"
        "s_add_i32 m0, s68, 4096\n\t"
        "s_add_i32 s69, s74, s20\n\t"
        "s_add_i32 s69, s69, 3072\n\t"
        "buffer_load_dwordx4 v120, s[36:39], s69 offen  lds\n\t"
        "v_fma_mix_f32 v16, v16, v84, v88 op_sel_hi:[1,0,0]\n\t"
        "v_fma_mix_f32 v17, v17, v85, v89 op_sel_hi:[1,0,0]\n\t"
        "v_fma_mix_f32 v18, v18, v86, v90 op_sel_hi:[1,0,0]\n\t"
        "v_fma_mix_f32 v19, v19, v87, v91 op_sel_hi:[1,0,0]\n\t"
        "v_cvt_pk_bf16_f32 v69, v12, v16\n\t"
        "v_cvt_pk_bf16_f32 v73, v13, v17\n\t"
        "v_cvt_pk_bf16_f32 v77, v14, v18\n\t"
        "v_cvt_pk_bf16_f32 v81, v15, v19\n\t"
        "s_waitcnt vmcnt(12)\n\t"
        "v_fma_mix_f32 v20, v20, v84, v88 op_sel_hi:[1,0,0]\n\t"
        "v_fma_mix_f32 v21, v21, v85, v89 op_sel_hi:[1,0,0]\n\t"
        "v_fma_mix_f32 v22, v22, v86, v90 op_sel_hi:[1,0,0]\n\t"
        "v_fma_mix_f32 v23, v23, v87, v91 op_sel_hi:[1,0,0]\n\t"
        "v_fma_mix_f32 v24, v24, v84, v88 op_sel_hi:[1,0,0]\n\t"
        "v_fma_mix_f32 v25, v25, v85, v89 op_sel_hi:[1,0,0]\n\t"
        "v_fma_mix_f32 v26, v26, v86, v90 op_sel_hi:[1,0,0]\n\t"
        "s_add_i32 s69, s74, s21\n\t"
        "s_add_i32 s69, s69, 2048\n\t"
        "buffer_load_dwordx4 v120, s[36:39], s69 offen offset:1024  lds\n\t"
        "v_fma_mix_f32 v27, v27, v87, v91 op_sel_hi:[1,0,0]\n\t"
        "v_cvt_pk_bf16_f32 v70, v20, v24\n\t"
        "v_cvt_pk_bf16_f32 v74, v21, v25\n\t"
        "v_cvt_pk_bf16_f32 v78, v22, v26\n\t"
        "v_cvt_pk_bf16_f32 v82, v23, v27\n\t"
        "s_waitcnt vmcnt(11)\n\t"
        "v_fma_mix_f32 v28, v28, v84, v88 op_sel_hi:[1,0,0]\n\t"
        "v_fma_mix_f32 v29, v29, v85, v89 op_sel_hi:[1,0,0]\n\t"
        "v_fma_mix_f32 v30, v30, v86, v90 op_sel_hi:[1,0,0]\n\t"
        "v_fma_mix_f32 v31, v31, v87, v91 op_sel_hi:[1,0,0]\n\t"
        "v_fma_mix_f32 v32, v32, v84, v88 op_sel_hi:[1,0,0]\n\t"
        "v_fma_mix_f32 v33, v33, v85, v89 op_sel_hi:[1,0,0]\n\t"
        "v_fma_mix_f32 v34, v34, v86, v90 op_sel_hi:[1,0,0]\n\t"
        "v_fma_mix_f32 v35, v35, v87, v91 op_sel_hi:[1,0,0]\n\t"
        "v_cvt_pk_bf16_f32 v71, v28, v32\n\t"
        "v_cvt_pk_bf16_f32 v75, v29, v33\n\t"
        "s_add_i32 s69, s74, s22\n\t"
        "s_add_i32 s69, s69, 1024\n\t"
        "buffer_load_dwordx4 v120, s[36:39], s69 offen offset:2048  lds\n\t"
        "v_cvt_pk_bf16_f32 v79, v30, v34\n\t"
        "v_cvt_pk_bf16_f32 v83, v31, v35\n\t"
        "ds_write_b128 v130, v[68:71] offset:0\n\t"
        "ds_write_b128 v130, v[72:75] offset:1024\n\t"
        "ds_write_b128 v130, v[76:79] offset:128\n\t"
        "ds_write_b128 v130, v[80:83] offset:1152\n\t"
        "s_add_i32 s69, s74, s23\n\t"
        "s_add_i32 s69, s69, 0\n\t"
        "buffer_load_dwordx4 v120, s[36:39], s69 offen offset:3072  lds\n\t"
        "s_add_i32 m0, s68, 8192\n\t"
        "s_add_i32 s69, s74, s24\n\t"
        "s_add_i32 s69, s69, 3072\n\t"
        "buffer_load_dwordx4 v120, s[36:39], s69 offen  lds\n\t"
        "s_add_i32 s74, s74, 36864\n\t"
        "s_cmp_ge_u32 s74, s94\n\t"
        "s_cselect_b32 s69, s93, 0\n\t"
        "s_sub_i32 s74, s74, s69\n\t"
        "s_waitcnt vmcnt(9)\n\t"
        "s_waitcnt lgkmcnt(0)\n\t"
        "s_barrier\n\t"
        "L_ppost_%=:\n\t"
        "s_waitcnt vmcnt(0)\n\t"
        "s_barrier\n\t"
        "s_mul_i32 s68, s76, 18432\n\t"
        "s_add_i32 s68, s68, s78\n\t"
        "s_add_i32 s68, s68, 0\n\t"
        "s_add_i32 m0, s68, 0\n\t"
        "s_add_i32 s69, s74, s16\n\t"
        "s_add_i32 s69, s69, 3072\n\t"
        "buffer_load_dwordx4 v120, s[36:39], s69 offen  lds\n\t"
        "s_add_i32 s69, s74, s17\n\t"
        "s_add_i32 s69, s69, 2048\n\t"
        "buffer_load_dwordx4 v120, s[36:39], s69 offen offset:1024  lds\n\t"
        "s_add_i32 s69, s74, s18\n\t"
        "s_add_i32 s69, s69, 1024\n\t"
        "buffer_load_dwordx4 v120, s[36:39], s69 offen offset:2048  lds\n\t"
        "s_add_i32 s69, s74, s19\n\t"
        "s_add_i32 s69, s69, 0\n\t"
        "buffer_load_dwordx4 v120, s[36:39], s69 offen offset:3072  lds\n\t"
        "s_add_i32 m0, s68, 4096\n\t"
        "s_add_i32 s69, s74, s20\n\t"
        "s_add_i32 s69, s69, 3072\n\t"
        "buffer_load_dwordx4 v120, s[36:39], s69 offen  lds\n\t"
        "s_add_i32 s69, s74, s21\n\t"
        "s_add_i32 s69, s69, 2048\n\t"
        "buffer_load_dwordx4 v120, s[36:39], s69 offen offset:1024  lds\n\t"
        "s_add_i32 s69, s74, s22\n\t"
        "s_add_i32 s69, s69, 1024\n\t"
        "buffer_load_dwordx4 v120, s[36:39], s69 offen offset:2048  lds\n\t"
        "s_add_i32 s69, s74, s23\n\t"
        "s_add_i32 s69, s69, 0\n\t"
        "buffer_load_dwordx4 v120, s[36:39], s69 offen offset:3072  lds\n\t"
        "s_add_i32 m0, s68, 8192\n\t"
        "s_add_i32 s69, s74, s24\n\t"
        "s_add_i32 s69, s69, 3072\n\t"
        "buffer_load_dwordx4 v120, s[36:39], s69 offen  lds\n\t"
        "s_add_i32 s74, s74, 36864\n\t"
        "s_cmp_ge_u32 s74, s94\n\t"
        "s_cselect_b32 s69, s93, 0\n\t"
        "s_sub_i32 s74, s74, s69\n\t"
        "s_cmp_eq_u32 s76, 0\n\t"
        "s_cbranch_scc0 L_st_%=\n\t"
        "s_add_i32 s68, s78, 36864\n\t"
        "s_add_i32 m0, s68, 0\n\t"
        "s_add_i32 s69, s74, s16\n\t"
        "s_add_i32 s69, s69, 3072\n\t"
        "buffer_load_dwordx4 v120, s[36:39], s69 offen  lds\n\t"
        "s_add_i32 s69, s74, s17\n\t"
        "s_add_i32 s69, s69, 2048\n\t"
        "buffer_load_dwordx4 v120, s[36:39], s69 offen offset:1024  lds\n\t"
        "s_add_i32 s69, s74, s18\n\t"
        "s_add_i32 s69, s69, 1024\n\t"
        "buffer_load_dwordx4 v120, s[36:39], s69 offen offset:2048  lds\n\t"
        "s_add_i32 s69, s74, s19\n\t"
        "s_add_i32 s69, s69, 0\n\t"
        "buffer_load_dwordx4 v120, s[36:39], s69 offen offset:3072  lds\n\t"
        "s_add_i32 m0, s68, 4096\n\t"
        "s_add_i32 s69, s74, s20\n\t"
        "s_add_i32 s69, s69, 3072\n\t"
        "buffer_load_dwordx4 v120, s[36:39], s69 offen  lds\n\t"
        "s_add_i32 s69, s74, s21\n\t"
        "s_add_i32 s69, s69, 2048\n\t"
        "buffer_load_dwordx4 v120, s[36:39], s69 offen offset:1024  lds\n\t"
        "s_add_i32 s69, s74, s22\n\t"
        "s_add_i32 s69, s69, 1024\n\t"
        "buffer_load_dwordx4 v120, s[36:39], s69 offen offset:2048  lds\n\t"
        "s_add_i32 s69, s74, s23\n\t"
        "s_add_i32 s69, s69, 0\n\t"
        "buffer_load_dwordx4 v120, s[36:39], s69 offen offset:3072  lds\n\t"
        "s_add_i32 m0, s68, 8192\n\t"
        "s_add_i32 s69, s74, s24\n\t"
        "s_add_i32 s69, s69, 3072\n\t"
        "buffer_load_dwordx4 v120, s[36:39], s69 offen  lds\n\t"
        "s_add_i32 s74, s74, 18432\n\t"
        "s_cmp_ge_u32 s74, s94\n\t"
        "s_cselect_b32 s69, s93, 0\n\t"
        "s_sub_i32 s74, s74, s69\n\t"
        "s_barrier\n\t"
        "s_add_i32 s68, s78, 55296\n\t"
        "s_add_i32 m0, s68, 0\n\t"
        "s_add_i32 s69, s74, s16\n\t"
        "s_add_i32 s69, s69, 3072\n\t"
        "buffer_load_dwordx4 v120, s[36:39], s69 offen  lds\n\t"
        "s_add_i32 s69, s74, s17\n\t"
        "s_add_i32 s69, s69, 2048\n\t"
        "buffer_load_dwordx4 v120, s[36:39], s69 offen offset:1024  lds\n\t"
        "s_add_i32 s69, s74, s18\n\t"
        "s_add_i32 s69, s69, 1024\n\t"
        "buffer_load_dwordx4 v120, s[36:39], s69 offen offset:2048  lds\n\t"
        "s_add_i32 s69, s74, s19\n\t"
        "s_add_i32 s69, s69, 0\n\t"
        "buffer_load_dwordx4 v120, s[36:39], s69 offen offset:3072  lds\n\t"
        "s_add_i32 m0, s68, 4096\n\t"
        "s_add_i32 s69, s74, s20\n\t"
        "s_add_i32 s69, s69, 3072\n\t"
        "buffer_load_dwordx4 v120, s[36:39], s69 offen  lds\n\t"
        "s_add_i32 s69, s74, s21\n\t"
        "s_add_i32 s69, s69, 2048\n\t"
        "buffer_load_dwordx4 v120, s[36:39], s69 offen offset:1024  lds\n\t"
        "s_add_i32 s69, s74, s22\n\t"
        "s_add_i32 s69, s69, 1024\n\t"
        "buffer_load_dwordx4 v120, s[36:39], s69 offen offset:2048  lds\n\t"
        "s_add_i32 s69, s74, s23\n\t"
        "s_add_i32 s69, s69, 0\n\t"
        "buffer_load_dwordx4 v120, s[36:39], s69 offen offset:3072  lds\n\t"
        "s_add_i32 m0, s68, 8192\n\t"
        "s_add_i32 s69, s74, s24\n\t"
        "s_add_i32 s69, s69, 3072\n\t"
        "buffer_load_dwordx4 v120, s[36:39], s69 offen  lds\n\t"
        "s_add_i32 s74, s74, 18432\n\t"
        "s_cmp_ge_u32 s74, s94\n\t"
        "s_cselect_b32 s69, s93, 0\n\t"
        "s_sub_i32 s74, s74, s69\n\t"
        "s_waitcnt vmcnt(18)\n\t"
        "s_barrier\n\t"
        "s_add_i32 s68, s78, 73728\n\t"
        "s_add_i32 m0, s68, 0\n\t"
        "s_add_i32 s69, s74, s16\n\t"
        "s_add_i32 s69, s69, 3072\n\t"
        "buffer_load_dwordx4 v120, s[36:39], s69 offen  lds\n\t"
        "s_add_i32 s69, s74, s17\n\t"
        "s_add_i32 s69, s69, 2048\n\t"
        "buffer_load_dwordx4 v120, s[36:39], s69 offen offset:1024  lds\n\t"
        "s_add_i32 s69, s74, s18\n\t"
        "s_add_i32 s69, s69, 1024\n\t"
        "buffer_load_dwordx4 v120, s[36:39], s69 offen offset:2048  lds\n\t"
        "s_add_i32 s69, s74, s19\n\t"
        "s_add_i32 s69, s69, 0\n\t"
        "buffer_load_dwordx4 v120, s[36:39], s69 offen offset:3072  lds\n\t"
        "s_add_i32 m0, s68, 4096\n\t"
        "s_add_i32 s69, s74, s20\n\t"
        "s_add_i32 s69, s69, 3072\n\t"
        "buffer_load_dwordx4 v120, s[36:39], s69 offen  lds\n\t"
        "s_add_i32 s69, s74, s21\n\t"
        "s_add_i32 s69, s69, 2048\n\t"
        "buffer_load_dwordx4 v120, s[36:39], s69 offen offset:1024  lds\n\t"
        "s_add_i32 s69, s74, s22\n\t"
        "s_add_i32 s69, s69, 1024\n\t"
        "buffer_load_dwordx4 v120, s[36:39], s69 offen offset:2048  lds\n\t"
        "s_add_i32 s69, s74, s23\n\t"
        "s_add_i32 s69, s69, 0\n\t"
        "buffer_load_dwordx4 v120, s[36:39], s69 offen offset:3072  lds\n\t"
        "s_add_i32 m0, s68, 8192\n\t"
        "s_add_i32 s69, s74, s24\n\t"
        "s_add_i32 s69, s69, 3072\n\t"
        "buffer_load_dwordx4 v120, s[36:39], s69 offen  lds\n\t"
        "s_add_i32 s74, s74, 18432\n\t"
        "s_cmp_ge_u32 s74, s94\n\t"
        "s_cselect_b32 s69, s93, 0\n\t"
        "s_sub_i32 s74, s74, s69\n\t"
        "s_barrier\n\t"
        "s_add_i32 s68, s78, 126976\n\t"
        "s_add_i32 m0, s68, 0\n\t"
        "s_add_i32 s69, s74, s16\n\t"
        "s_add_i32 s69, s69, 3072\n\t"
        "buffer_load_dwordx4 v120, s[36:39], s69 offen  lds\n\t"
        "s_add_i32 s69, s74, s17\n\t"
        "s_add_i32 s69, s69, 2048\n\t"
        "buffer_load_dwordx4 v120, s[36:39], s69 offen offset:1024  lds\n\t"
        "s_add_i32 s69, s74, s18\n\t"
        "s_add_i32 s69, s69, 1024\n\t"
        "buffer_load_dwordx4 v120, s[36:39], s69 offen offset:2048  lds\n\t"
        "s_add_i32 s69, s74, s19\n\t"
        "s_add_i32 s69, s69, 0\n\t"
        "buffer_load_dwordx4 v120, s[36:39], s69 offen offset:3072  lds\n\t"
        "s_add_i32 m0, s68, 4096\n\t"
        "s_add_i32 s69, s74, s20\n\t"
        "s_add_i32 s69, s69, 3072\n\t"
        "buffer_load_dwordx4 v120, s[36:39], s69 offen  lds\n\t"
        "s_add_i32 s69, s74, s21\n\t"
        "s_add_i32 s69, s69, 2048\n\t"
        "buffer_load_dwordx4 v120, s[36:39], s69 offen offset:1024  lds\n\t"
        "s_add_i32 s69, s74, s22\n\t"
        "s_add_i32 s69, s69, 1024\n\t"
        "buffer_load_dwordx4 v120, s[36:39], s69 offen offset:2048  lds\n\t"
        "s_add_i32 s69, s74, s23\n\t"
        "s_add_i32 s69, s69, 0\n\t"
        "buffer_load_dwordx4 v120, s[36:39], s69 offen offset:3072  lds\n\t"
        "s_add_i32 m0, s68, 8192\n\t"
        "s_add_i32 s69, s74, s24\n\t"
        "s_add_i32 s69, s69, 3072\n\t"
        "buffer_load_dwordx4 v120, s[36:39], s69 offen  lds\n\t"
        "s_add_i32 s74, s74, 18432\n\t"
        "s_cmp_ge_u32 s74, s94\n\t"
        "s_cselect_b32 s69, s93, 0\n\t"
        "s_sub_i32 s74, s74, s69\n\t"
        "s_waitcnt vmcnt(27)\n\t"
        "s_barrier\n\t"
        "s_add_i32 s68, s78, 145408\n\t"
        "s_add_i32 s74, s74, s79\n\t"
        "s_add_i32 m0, s68, 0\n\t"
        "s_add_i32 s69, s74, s16\n\t"
        "s_add_i32 s69, s69, 3072\n\t"
        "buffer_load_dwordx4 v120, s[36:39], s69 offen  lds\n\t"
        "s_add_i32 s69, s74, s17\n\t"
        "s_add_i32 s69, s69, 2048\n\t"
        "buffer_load_dwordx4 v120, s[36:39], s69 offen offset:1024  lds\n\t"
        "s_add_i32 s69, s74, s18\n\t"
        "s_add_i32 s69, s69, 1024\n\t"
        "buffer_load_dwordx4 v120, s[36:39], s69 offen offset:2048  lds\n\t"
        "s_add_i32 s69, s74, s19\n\t"
        "s_add_i32 s69, s69, 0\n\t"
        "buffer_load_dwordx4 v120, s[36:39], s69 offen offset:3072  lds\n\t"
        "s_add_i32 m0, s68, 4096\n\t"
        "s_add_i32 s69, s74, s20\n\t"
        "s_add_i32 s69, s69, 3072\n\t"
        "buffer_load_dwordx4 v120, s[36:39], s69 offen  lds\n\t"
        "s_add_i32 s69, s74, s21\n\t"
        "s_add_i32 s69, s69, 2048\n\t"
        "buffer_load_dwordx4 v120, s[36:39], s69 offen offset:1024  lds\n\t"
        "s_add_i32 s69, s74, s22\n\t"
        "s_add_i32 s69, s69, 1024\n\t"
        "buffer_load_dwordx4 v120, s[36:39], s69 offen offset:2048  lds\n\t"
        "s_add_i32 s69, s74, s23\n\t"
        "s_add_i32 s69, s69, 0\n\t"
        "buffer_load_dwordx4 v120, s[36:39], s69 offen offset:3072  lds\n\t"
        "s_add_i32 m0, s68, 8192\n\t"
        "s_add_i32 s69, s74, s24\n\t"
        "s_add_i32 s69, s69, 3072\n\t"
        "buffer_load_dwordx4 v120, s[36:39], s69 offen  lds\n\t"
        "s_waitcnt vmcnt(27)\n\t"
        "s_barrier\n\t"
        "s_waitcnt vmcnt(18)\n\t"
        "s_barrier\n\t"
        "s_waitcnt vmcnt(9)\n\t"
        "s_barrier\n\t"
        "s_waitcnt vmcnt(0)\n\t"
        "s_barrier\n\t"
        "s_cmp_eq_u32 s60, 19\n\t"
        "s_cbranch_scc0 L_ld18_%=\n\t"
        "s_barrier\n\t"
        "L_ld18_%=:\n\t"
        "s_barrier\n\t"
        "s_endpgm\n\t"
        "L_st_%=:\n\t"
        "s_barrier\n\t"
        "ds_read_b64 v[140:141], v135 offset:0\n\t"
        "ds_read_b64 v[142:143], v136 offset:0\n\t"
        "ds_read_b64 v[144:145], v135 offset:1024\n\t"
        "ds_read_b64 v[146:147], v136 offset:1024\n\t"
        "ds_read_b64 v[148:149], v135 offset:4096\n\t"
        "ds_read_b64 v[150:151], v136 offset:4096\n\t"
        "ds_read_b64 v[152:153], v135 offset:5120\n\t"
        "ds_read_b64 v[154:155], v136 offset:5120\n\t"
        "s_add_i32 s64, s77, 0\n\t"
        "s_waitcnt lgkmcnt(6)\n\t"
        "buffer_store_dwordx4 v[140:143], v134, s[52:55], s64 offen sc1 nt\n\t"
        "s_waitcnt lgkmcnt(4)\n\t"
        "buffer_store_dwordx4 v[144:147], v137, s[52:55], s64 offen sc1 nt\n\t"
        "s_add_i32 s64, s77, 147456\n\t"
        "s_waitcnt lgkmcnt(2)\n\t"
        "buffer_store_dwordx4 v[148:151], v134, s[52:55], s64 offen sc1 nt\n\t"
        "s_waitcnt lgkmcnt(0)\n\t"
        "buffer_store_dwordx4 v[152:155], v137, s[52:55], s64 offen sc1 nt\n\t"
        "s_waitcnt vmcnt(4)\n\t"
        "s_barrier\n\t"
        "ds_read_b64 v[140:141], v135 offset:8192\n\t"
        "ds_read_b64 v[142:143], v136 offset:8192\n\t"
        "ds_read_b64 v[144:145], v135 offset:9216\n\t"
        "ds_read_b64 v[146:147], v136 offset:9216\n\t"
        "ds_read_b64 v[148:149], v135 offset:12288\n\t"
        "ds_read_b64 v[150:151], v136 offset:12288\n\t"
        "ds_read_b64 v[152:153], v135 offset:13312\n\t"
        "ds_read_b64 v[154:155], v136 offset:13312\n\t"
        "s_add_i32 s64, s77, 294912\n\t"
        "s_waitcnt lgkmcnt(6)\n\t"
        "buffer_store_dwordx4 v[140:143], v134, s[52:55], s64 offen sc1 nt\n\t"
        "s_waitcnt lgkmcnt(4)\n\t"
        "buffer_store_dwordx4 v[144:147], v137, s[52:55], s64 offen sc1 nt\n\t"
        "s_mov_b32 s64, s92\n\t"
        "s_add_i32 s92, s92, 147456\n\t"
        "s_cmp_ge_u32 s92, s95\n\t"
        "s_cselect_b32 s65, s96, 0\n\t"
        "s_sub_i32 s92, s92, s65\n\t"
        "s_waitcnt lgkmcnt(2)\n\t"
        "buffer_store_dwordx4 v[148:151], v134, s[52:55], s64 offen sc1 nt\n\t"
        "s_waitcnt lgkmcnt(0)\n\t"
        "buffer_store_dwordx4 v[152:155], v137, s[52:55], s64 offen sc1 nt\n\t"
        "s_barrier\n\t"
        "ds_read_b64 v[140:141], v135 offset:0\n\t"
        "ds_read_b64 v[142:143], v136 offset:0\n\t"
        "ds_read_b64 v[144:145], v135 offset:1024\n\t"
        "ds_read_b64 v[146:147], v136 offset:1024\n\t"
        "ds_read_b64 v[148:149], v135 offset:4096\n\t"
        "ds_read_b64 v[150:151], v136 offset:4096\n\t"
        "ds_read_b64 v[152:153], v135 offset:5120\n\t"
        "ds_read_b64 v[154:155], v136 offset:5120\n\t"
        "s_add_i32 s64, s77, 442368\n\t"
        "s_waitcnt lgkmcnt(6)\n\t"
        "buffer_store_dwordx4 v[140:143], v134, s[52:55], s64 offen sc1 nt\n\t"
        "s_waitcnt lgkmcnt(4)\n\t"
        "buffer_store_dwordx4 v[144:147], v137, s[52:55], s64 offen sc1 nt\n\t"
        "s_mov_b32 s64, s92\n\t"
        "s_add_i32 s92, s92, 147456\n\t"
        "s_cmp_ge_u32 s92, s95\n\t"
        "s_cselect_b32 s65, s96, 0\n\t"
        "s_sub_i32 s92, s92, s65\n\t"
        "s_waitcnt lgkmcnt(2)\n\t"
        "buffer_store_dwordx4 v[148:151], v134, s[52:55], s64 offen sc1 nt\n\t"
        "s_waitcnt lgkmcnt(0)\n\t"
        "buffer_store_dwordx4 v[152:155], v137, s[52:55], s64 offen sc1 nt\n\t"
        "s_barrier\n\t"
        "ds_read_b64 v[140:141], v135 offset:8192\n\t"
        "ds_read_b64 v[142:143], v136 offset:8192\n\t"
        "ds_read_b64 v[144:145], v135 offset:9216\n\t"
        "ds_read_b64 v[146:147], v136 offset:9216\n\t"
        "ds_read_b64 v[148:149], v135 offset:12288\n\t"
        "ds_read_b64 v[150:151], v136 offset:12288\n\t"
        "ds_read_b64 v[152:153], v135 offset:13312\n\t"
        "ds_read_b64 v[154:155], v136 offset:13312\n\t"
        "s_add_i32 s64, s77, 589824\n\t"
        "s_waitcnt lgkmcnt(6)\n\t"
        "buffer_store_dwordx4 v[140:143], v134, s[52:55], s64 offen sc1 nt\n\t"
        "s_waitcnt lgkmcnt(4)\n\t"
        "buffer_store_dwordx4 v[144:147], v137, s[52:55], s64 offen sc1 nt\n\t"
        "s_mov_b32 s64, s92\n\t"
        "s_add_i32 s92, s92, 147456\n\t"
        "s_cmp_ge_u32 s92, s95\n\t"
        "s_cselect_b32 s65, s96, 0\n\t"
        "s_sub_i32 s92, s92, s65\n\t"
        "s_waitcnt lgkmcnt(2)\n\t"
        "buffer_store_dwordx4 v[148:151], v134, s[52:55], s64 offen sc1 nt\n\t"
        "s_waitcnt lgkmcnt(0)\n\t"
        "buffer_store_dwordx4 v[152:155], v137, s[52:55], s64 offen sc1 nt\n\t"
        "s_barrier\n\t"
        "ds_read_b64 v[140:141], v135 offset:0\n\t"
        "ds_read_b64 v[142:143], v136 offset:0\n\t"
        "ds_read_b64 v[144:145], v135 offset:1024\n\t"
        "ds_read_b64 v[146:147], v136 offset:1024\n\t"
        "ds_read_b64 v[148:149], v135 offset:4096\n\t"
        "ds_read_b64 v[150:151], v136 offset:4096\n\t"
        "ds_read_b64 v[152:153], v135 offset:5120\n\t"
        "ds_read_b64 v[154:155], v136 offset:5120\n\t"
        "s_add_i32 s64, s77, 737280\n\t"
        "s_waitcnt lgkmcnt(6)\n\t"
        "buffer_store_dwordx4 v[140:143], v134, s[52:55], s64 offen sc1 nt\n\t"
        "s_waitcnt lgkmcnt(4)\n\t"
        "buffer_store_dwordx4 v[144:147], v137, s[52:55], s64 offen sc1 nt\n\t"
        "s_mov_b32 s64, s92\n\t"
        "s_add_i32 s92, s92, 147456\n\t"
        "s_cmp_ge_u32 s92, s95\n\t"
        "s_cselect_b32 s65, s96, 0\n\t"
        "s_sub_i32 s92, s92, s65\n\t"
        "s_waitcnt lgkmcnt(2)\n\t"
        "buffer_store_dwordx4 v[148:151], v134, s[52:55], s64 offen sc1 nt\n\t"
        "s_waitcnt lgkmcnt(0)\n\t"
        "buffer_store_dwordx4 v[152:155], v137, s[52:55], s64 offen sc1 nt\n\t"
        "s_barrier\n\t"
        "ds_read_b64 v[140:141], v135 offset:8192\n\t"
        "ds_read_b64 v[142:143], v136 offset:8192\n\t"
        "ds_read_b64 v[144:145], v135 offset:9216\n\t"
        "ds_read_b64 v[146:147], v136 offset:9216\n\t"
        "ds_read_b64 v[148:149], v135 offset:12288\n\t"
        "ds_read_b64 v[150:151], v136 offset:12288\n\t"
        "ds_read_b64 v[152:153], v135 offset:13312\n\t"
        "ds_read_b64 v[154:155], v136 offset:13312\n\t"
        "s_add_i32 s64, s77, 884736\n\t"
        "s_waitcnt lgkmcnt(6)\n\t"
        "buffer_store_dwordx4 v[140:143], v134, s[52:55], s64 offen sc1 nt\n\t"
        "s_waitcnt lgkmcnt(4)\n\t"
        "buffer_store_dwordx4 v[144:147], v137, s[52:55], s64 offen sc1 nt\n\t"
        "s_mov_b32 s64, s92\n\t"
        "s_add_i32 s92, s92, 147456\n\t"
        "s_cmp_ge_u32 s92, s95\n\t"
        "s_cselect_b32 s65, s96, 0\n\t"
        "s_sub_i32 s92, s92, s65\n\t"
        "s_waitcnt lgkmcnt(2)\n\t"
        "buffer_store_dwordx4 v[148:151], v134, s[52:55], s64 offen sc1 nt\n\t"
        "s_waitcnt lgkmcnt(0)\n\t"
        "buffer_store_dwordx4 v[152:155], v137, s[52:55], s64 offen sc1 nt\n\t"
        "s_barrier\n\t"
        "ds_read_b64 v[140:141], v135 offset:0\n\t"
        "ds_read_b64 v[142:143], v136 offset:0\n\t"
        "ds_read_b64 v[144:145], v135 offset:1024\n\t"
        "ds_read_b64 v[146:147], v136 offset:1024\n\t"
        "ds_read_b64 v[148:149], v135 offset:4096\n\t"
        "ds_read_b64 v[150:151], v136 offset:4096\n\t"
        "ds_read_b64 v[152:153], v135 offset:5120\n\t"
        "ds_read_b64 v[154:155], v136 offset:5120\n\t"
        "s_add_i32 s64, s77, 1032192\n\t"
        "s_waitcnt lgkmcnt(6)\n\t"
        "buffer_store_dwordx4 v[140:143], v134, s[52:55], s64 offen sc1 nt\n\t"
        "s_waitcnt lgkmcnt(4)\n\t"
        "buffer_store_dwordx4 v[144:147], v137, s[52:55], s64 offen sc1 nt\n\t"
        "s_mov_b32 s64, s92\n\t"
        "s_add_i32 s92, s92, 147456\n\t"
        "s_cmp_ge_u32 s92, s95\n\t"
        "s_cselect_b32 s65, s96, 0\n\t"
        "s_sub_i32 s92, s92, s65\n\t"
        "s_waitcnt lgkmcnt(2)\n\t"
        "buffer_store_dwordx4 v[148:151], v134, s[52:55], s64 offen sc1 nt\n\t"
        "s_waitcnt lgkmcnt(0)\n\t"
        "buffer_store_dwordx4 v[152:155], v137, s[52:55], s64 offen sc1 nt\n\t"
        "s_barrier\n\t"
        "ds_read_b64 v[140:141], v135 offset:8192\n\t"
        "ds_read_b64 v[142:143], v136 offset:8192\n\t"
        "ds_read_b64 v[144:145], v135 offset:9216\n\t"
        "ds_read_b64 v[146:147], v136 offset:9216\n\t"
        "ds_read_b64 v[148:149], v135 offset:12288\n\t"
        "ds_read_b64 v[150:151], v136 offset:12288\n\t"
        "ds_read_b64 v[152:153], v135 offset:13312\n\t"
        "ds_read_b64 v[154:155], v136 offset:13312\n\t"
        "s_add_i32 s64, s77, 1179648\n\t"
        "s_waitcnt lgkmcnt(6)\n\t"
        "buffer_store_dwordx4 v[140:143], v134, s[52:55], s64 offen sc1 nt\n\t"
        "s_waitcnt lgkmcnt(4)\n\t"
        "buffer_store_dwordx4 v[144:147], v137, s[52:55], s64 offen sc1 nt\n\t"
        "s_mov_b32 s64, s92\n\t"
        "s_add_i32 s92, s92, 147456\n\t"
        "s_cmp_ge_u32 s92, s95\n\t"
        "s_cselect_b32 s65, s96, 0\n\t"
        "s_sub_i32 s92, s92, s65\n\t"
        "s_waitcnt lgkmcnt(2)\n\t"
        "buffer_store_dwordx4 v[148:151], v134, s[52:55], s64 offen sc1 nt\n\t"
        "s_waitcnt lgkmcnt(0)\n\t"
        "buffer_store_dwordx4 v[152:155], v137, s[52:55], s64 offen sc1 nt\n\t"
        "s_cmp_eq_u32 s60, 19\n\t"
        "s_cbranch_scc1 L_p19_%=\n\t"
        "s_barrier\n\t"
        "ds_read_b64 v[140:141], v135 offset:0\n\t"
        "ds_read_b64 v[142:143], v136 offset:0\n\t"
        "ds_read_b64 v[144:145], v135 offset:1024\n\t"
        "ds_read_b64 v[146:147], v136 offset:1024\n\t"
        "ds_read_b64 v[148:149], v135 offset:4096\n\t"
        "ds_read_b64 v[150:151], v136 offset:4096\n\t"
        "ds_read_b64 v[152:153], v135 offset:5120\n\t"
        "ds_read_b64 v[154:155], v136 offset:5120\n\t"
        "s_add_i32 s64, s77, 1327104\n\t"
        "s_waitcnt lgkmcnt(6)\n\t"
        "buffer_store_dwordx4 v[140:143], v134, s[52:55], s64 offen sc1 nt\n\t"
        "s_waitcnt lgkmcnt(4)\n\t"
        "buffer_store_dwordx4 v[144:147], v137, s[52:55], s64 offen sc1 nt\n\t"
        "s_mov_b32 s64, s92\n\t"
        "s_add_i32 s92, s92, 147456\n\t"
        "s_cmp_ge_u32 s92, s95\n\t"
        "s_cselect_b32 s65, s96, 0\n\t"
        "s_sub_i32 s92, s92, s65\n\t"
        "s_waitcnt lgkmcnt(2)\n\t"
        "buffer_store_dwordx4 v[148:151], v134, s[52:55], s64 offen sc1 nt\n\t"
        "s_waitcnt lgkmcnt(0)\n\t"
        "buffer_store_dwordx4 v[152:155], v137, s[52:55], s64 offen sc1 nt\n\t"
        "s_endpgm\n\t"
        "L_p19_%=:\n\t"
        "s_barrier\n\t"
        "ds_read_b64 v[140:141], v135 offset:0\n\t"
        "ds_read_b64 v[142:143], v136 offset:0\n\t"
        "ds_read_b64 v[144:145], v135 offset:1024\n\t"
        "ds_read_b64 v[146:147], v136 offset:1024\n\t"
        "ds_read_b64 v[148:149], v135 offset:4096\n\t"
        "ds_read_b64 v[150:151], v136 offset:4096\n\t"
        "ds_read_b64 v[152:153], v135 offset:5120\n\t"
        "ds_read_b64 v[154:155], v136 offset:5120\n\t"
        "s_add_i32 s64, s77, 1327104\n\t"
        "s_waitcnt lgkmcnt(6)\n\t"
        "buffer_store_dwordx4 v[140:143], v134, s[52:55], s64 offen sc1 nt\n\t"
        "s_waitcnt lgkmcnt(4)\n\t"
        "buffer_store_dwordx4 v[144:147], v137, s[52:55], s64 offen sc1 nt\n\t"
        "s_mov_b32 s64, s92\n\t"
        "s_add_i32 s92, s92, 147456\n\t"
        "s_cmp_ge_u32 s92, s95\n\t"
        "s_cselect_b32 s65, s96, 0\n\t"
        "s_sub_i32 s92, s92, s65\n\t"
        "s_waitcnt lgkmcnt(2)\n\t"
        "buffer_store_dwordx4 v[148:151], v134, s[52:55], s64 offen sc1 nt\n\t"
        "s_waitcnt lgkmcnt(0)\n\t"
        "buffer_store_dwordx4 v[152:155], v137, s[52:55], s64 offen sc1 nt\n\t"
        "s_barrier\n\t"
        "ds_read_b64 v[140:141], v135 offset:8192\n\t"
        "ds_read_b64 v[142:143], v136 offset:8192\n\t"
        "ds_read_b64 v[144:145], v135 offset:9216\n\t"
        "ds_read_b64 v[146:147], v136 offset:9216\n\t"
        "s_mov_b32 s64, s92\n\t"
        "s_add_i32 s92, s92, 147456\n\t"
        "s_cmp_ge_u32 s92, s95\n\t"
        "s_cselect_b32 s65, s96, 0\n\t"
        "s_sub_i32 s92, s92, s65\n\t"
        "s_waitcnt lgkmcnt(2)\n\t"
        "buffer_store_dwordx4 v[140:143], v134, s[52:55], s64 offen sc1 nt\n\t"
        "s_waitcnt lgkmcnt(0)\n\t"
        "buffer_store_dwordx4 v[144:147], v137, s[52:55], s64 offen sc1 nt\n\t"
        "s_endpgm\n\t"
        "L_cons_%=:\n\t"
        "v_and_b32 v4, 15, v0\n\t"
        "v_lshrrev_b32 v5, 4, v0\n\t"
        "v_lshrrev_b32 v6, 1, v4\n\t"
        "v_xor_b32 v7, v5, v6\n\t"
        "v_lshlrev_b32 v7, 4, v7\n\t"
        "v_and_b32 v8, 7, v4\n\t"
        "v_lshl_add_u32 v7, v8, 7, v7\n\t"
        "v_lshrrev_b32 v9, 3, v4\n\t"
        "v_mul_u32_u24 v10, 5120, v9\n\t"
        "v_add_u32 v1, v7, v10\n\t"
        "v_xor_b32 v2, 64, v1\n\t"
        "v_mul_u32_u24 v10, 9216, v9\n\t"
        "v_add_u32 v255, v7, v10\n\t"
        "v_and_b32 v10, 1, v4\n\t"
        "v_lshl_add_u32 v10, v10, 3, v6\n\t"
        "v_lshl_add_u32 v10, v5, 4, v10\n\t"
        "v_lshlrev_b32 v3, 4, v10\n\t"
        "s_lshl_b32 s62, s56, 11\n\t"
        "s_add_i32 s63, s62, 61440\n\t"
        "v_add_u32 v3, s63, v3\n\t"
        "s_lshl_b32 s63, s56, 2\n\t"
        "v_add_u32 v10, s63, v5\n\t"
        "v_xor_b32 v10, v10, v8\n\t"
        "v_lshlrev_b32 v10, 4, v10\n\t"
        "v_lshl_add_u32 v253, v4, 8, v10\n\t"
        "v_add_u32 v253, 92160, v253\n\t"
        "v_lshl_add_u32 v253, v9, 3, v253\n\t"
        "v_xor_b32 v254, 8, v253\n\t"
        "s_waitcnt lgkmcnt(0)\n\t"
        "s_getpc_b64 s[28:29]\n\t"
        "L_pc2_%=:\n\t"
        "s_sub_u32 s28, s28, (L_pc2_%=-L_begin_%=)\n\t"
        "s_subb_u32 s29, s29, 0\n\t"
        "s_and_b32 s28, s28, 0xffffff00\n\t"
        "s_and_b32 s29, s29, 0xffff\n\t"
        "s_mov_b32 s30, (L_end_%=-L_begin_%=)\n\t"
        "s_lshr_b32 s70, %[blk], 3\n\t"
        "s_and_b32 s70, s70, 15\n\t"
        "s_cmp_eq_u32 s70, 0\n\t"
        "s_cselect_b32 s30, s30, 0\n\t"
        "s_mov_b32 s31, 0x20000\n\t"
        "v_lshlrev_b32 v0, 4, v0\n\t"
        "s_mov_b32 m0, 121856\n\t"
        "s_nop 0\n\t"
        "s_cmp_eq_u32 s56, 0\n\t"
        "s_cselect_b32 s70, ((L_pit0_%=-L_begin_%=)&0xffffff80)+0, s70\n\t"
        "s_cmp_eq_u32 s56, 1\n\t"
        "s_cselect_b32 s70, ((L_pit1_%=-L_begin_%=)&0xffffff80)+0, s70\n\t"
        "s_cmp_eq_u32 s56, 2\n\t"
        "s_cselect_b32 s70, ((L_cit0_%=-L_begin_%=)&0xffffff80)+0, s70\n\t"
        "s_cmp_eq_u32 s56, 3\n\t"
        "s_cselect_b32 s70, ((L_cit1_%=-L_begin_%=)&0xffffff80)+128, s70\n\t"
        "buffer_load_dwordx4 v0, s[28:31], s70 offen lds\n\t"
        "s_cmp_eq_u32 s56, 0\n\t"
        "s_cselect_b32 s70, ((L_pit1_%=-L_begin_%=)&0xffffff80)+896, s70\n\t"
        "s_cmp_eq_u32 s56, 1\n\t"
        "s_cselect_b32 s70, ((L_pit2_%=-L_begin_%=)&0xffffff80)+768, s70\n\t"
        "s_cmp_eq_u32 s56, 2\n\t"
        "s_cselect_b32 s70, ((L_cit2_%=-L_begin_%=)&0xffffff80)+512, s70\n\t"
        "s_cmp_eq_u32 s56, 3\n\t"
        "s_cselect_b32 s70, ((L_pit0_%=-L_begin_%=)&0xffffff80)+1024, s70\n\t"
        "buffer_load_dwordx4 v0, s[28:31], s70 offen lds\n\t"
        "L_cit0_%=:\n\t"
        "s_barrier\n\t"
        "ds_read_b128 v[4:7], v3 offset:0\n\t"
        "ds_read_b128 v[8:11], v3 offset:1024\n\t"
        "ds_read_b128 v[228:231], v1 offset:0\n\t"
        "ds_read_b128 v[232:235], v1 offset:10240\n\t"
        "ds_read_b128 v[12:15], v3 offset:8192\n\t"
        "ds_read_b128 v[16:19], v3 offset:9216\n\t"
        "ds_read_b128 v[236:239], v1 offset:1024\n\t"
        "ds_read_b128 v[240:243], v1 offset:11264\n\t"
        "ds_read_b128 v[244:247], v1 offset:2048\n\t"
        "s_waitcnt lgkmcnt(5)\n\t"
        "v_mfma_f32_16x16x32_bf16 v[148:151], v[4:7], v[228:231], 0\n\t"
        "ds_read_b128 v[248:251], v1 offset:12288\n\t"
        "v_mfma_f32_16x16x32_bf16 v[152:155], v[8:11], v[228:231], 0\n\t"
        "v_mfma_f32_16x16x32_bf16 v[156:159], v[4:7], v[232:235], 0\n\t"
        "ds_read_b128 v[228:231], v1 offset:3072\n\t"
        "v_mfma_f32_16x16x32_bf16 v[160:163], v[8:11], v[232:235], 0\n\t"
        "s_waitcnt lgkmcnt(3)\n\t"
        "v_mfma_f32_16x16x32_bf16 v[164:167], v[4:7], v[236:239], 0\n\t"
        "ds_read_b128 v[232:235], v1 offset:13312\n\t"
        "v_mfma_f32_16x16x32_bf16 v[168:171], v[8:11], v[236:239], 0\n\t"
        "v_mfma_f32_16x16x32_bf16 v[172:175], v[4:7], v[240:243], 0\n\t"
        "ds_read_b128 v[236:239], v1 offset:4096\n\t"
        "v_mfma_f32_16x16x32_bf16 v[176:179], v[8:11], v[240:243], 0\n\t"
        "s_waitcnt lgkmcnt(3)\n\t"
        "v_mfma_f32_16x16x32_bf16 v[180:183], v[4:7], v[244:247], 0\n\t"
        "ds_read_b128 v[240:243], v1 offset:14336\n\t"
        "v_mfma_f32_16x16x32_bf16 v[184:187], v[8:11], v[244:247], 0\n\t"
        "v_mfma_f32_16x16x32_bf16 v[188:191], v[4:7], v[248:251], 0\n\t"
        "ds_read_b128 v[244:247], v2 offset:0\n\t"
        "v_mfma_f32_16x16x32_bf16 v[192:195], v[8:11], v[248:251], 0\n\t"
        "s_waitcnt lgkmcnt(3)\n\t"
        "v_mfma_f32_16x16x32_bf16 v[196:199], v[4:7], v[228:231], 0\n\t"
        "ds_read_b128 v[248:251], v2 offset:10240\n\t"
        "v_mfma_f32_16x16x32_bf16 v[200:203], v[8:11], v[228:231], 0\n\t"
        "v_mfma_f32_16x16x32_bf16 v[204:207], v[4:7], v[232:235], 0\n\t"
        "ds_read_b128 v[228:231], v2 offset:1024\n\t"
        "v_mfma_f32_16x16x32_bf16 v[208:211], v[8:11], v[232:235], 0\n\t"
        "s_waitcnt lgkmcnt(3)\n\t"
        "v_mfma_f32_16x16x32_bf16 v[212:215], v[4:7], v[236:239], 0\n\t"
        "ds_read_b128 v[232:235], v2 offset:11264\n\t"
        "v_mfma_f32_16x16x32_bf16 v[216:219], v[8:11], v[236:239], 0\n\t"
        "v_mfma_f32_16x16x32_bf16 v[220:223], v[4:7], v[240:243], 0\n\t"
        "ds_read_b128 v[236:239], v2 offset:2048\n\t"
        "v_mfma_f32_16x16x32_bf16 v[224:227], v[8:11], v[240:243], 0\n\t"
        "s_waitcnt lgkmcnt(3)\n\t"
        "v_mfma_f32_16x16x32_bf16 v[148:151], v[12:15], v[244:247], v[148:151]\n\t"
        "ds_read_b128 v[240:243], v2 offset:12288\n\t"
        "v_mfma_f32_16x16x32_bf16 v[152:155], v[16:19], v[244:247], v[152:155]\n\t"
        "v_mfma_f32_16x16x32_bf16 v[156:159], v[12:15], v[248:251], v[156:159]\n\t"
        "ds_read_b128 v[244:247], v2 offset:3072\n\t"
        "v_mfma_f32_16x16x32_bf16 v[160:163], v[16:19], v[248:251], v[160:163]\n\t"
        "s_waitcnt lgkmcnt(3)\n\t"
        "v_mfma_f32_16x16x32_bf16 v[164:167], v[12:15], v[228:231], v[164:167]\n\t"
        "ds_read_b128 v[248:251], v2 offset:13312\n\t"
        "v_mfma_f32_16x16x32_bf16 v[168:171], v[16:19], v[228:231], v[168:171]\n\t"
        "v_mfma_f32_16x16x32_bf16 v[172:175], v[12:15], v[232:235], v[172:175]\n\t"
        "ds_read_b128 v[228:231], v2 offset:4096\n\t"
        "v_mfma_f32_16x16x32_bf16 v[176:179], v[16:19], v[232:235], v[176:179]\n\t"
        "s_waitcnt lgkmcnt(3)\n\t"
        "v_mfma_f32_16x16x32_bf16 v[180:183], v[12:15], v[236:239], v[180:183]\n\t"
        "ds_read_b128 v[232:235], v2 offset:14336\n\t"
        "v_mfma_f32_16x16x32_bf16 v[184:187], v[16:19], v[236:239], v[184:187]\n\t"
        "v_mfma_f32_16x16x32_bf16 v[188:191], v[12:15], v[240:243], v[188:191]\n\t"
        "v_mfma_f32_16x16x32_bf16 v[192:195], v[16:19], v[240:243], v[192:195]\n\t"
        "s_waitcnt lgkmcnt(2)\n\t"
        "v_mfma_f32_16x16x32_bf16 v[196:199], v[12:15], v[244:247], v[196:199]\n\t"
        "v_mfma_f32_16x16x32_bf16 v[200:203], v[16:19], v[244:247], v[200:203]\n\t"
        "v_mfma_f32_16x16x32_bf16 v[204:207], v[12:15], v[248:251], v[204:207]\n\t"
        "v_mfma_f32_16x16x32_bf16 v[208:211], v[16:19], v[248:251], v[208:211]\n\t"
        "s_waitcnt lgkmcnt(0)\n\t"
        "v_mfma_f32_16x16x32_bf16 v[212:215], v[12:15], v[228:231], v[212:215]\n\t"
        "v_mfma_f32_16x16x32_bf16 v[216:219], v[16:19], v[228:231], v[216:219]\n\t"
        "v_mfma_f32_16x16x32_bf16 v[220:223], v[12:15], v[232:235], v[220:223]\n\t"
        "v_mfma_f32_16x16x32_bf16 v[224:227], v[16:19], v[232:235], v[224:227]\n\t"
        "s_cmp_eq_u32 s56, 0\n\t"
        "s_cselect_b32 s70, ((L_pit3_%=-L_begin_%=)&0xffffff80)+0, s70\n\t"
        "s_cmp_eq_u32 s56, 1\n\t"
        "s_cselect_b32 s70, ((L_cit3_%=-L_begin_%=)&0xffffff80)+0, s70\n\t"
        "s_cmp_eq_u32 s56, 2\n\t"
        "s_cselect_b32 s70, ((L_pit3_%=-L_begin_%=)&0xffffff80)+896, s70\n\t"
        "s_cmp_eq_u32 s56, 3\n\t"
        "s_cselect_b32 s70, ((L_cit4_%=-L_begin_%=)&0xffffff80)+0, s70\n\t"
        "buffer_load_dwordx4 v0, s[28:31], s70 offen lds\n\t"
        "L_cit1_%=:\n\t"
        "s_barrier\n\t"
        "ds_read_b128 v[20:23], v3 offset:16384\n\t"
        "ds_read_b128 v[24:27], v3 offset:17408\n\t"
        "ds_read_b128 v[228:231], v1 offset:20480\n\t"
        "ds_read_b128 v[232:235], v1 offset:30720\n\t"
        "ds_read_b128 v[28:31], v3 offset:24576\n\t"
        "ds_read_b128 v[32:35], v3 offset:25600\n\t"
        "ds_read_b128 v[236:239], v1 offset:21504\n\t"
        "ds_read_b128 v[240:243], v1 offset:31744\n\t"
        "ds_read_b128 v[244:247], v1 offset:22528\n\t"
        "s_waitcnt lgkmcnt(5)\n\t"
        "v_mfma_f32_16x16x32_bf16 v[148:151], v[20:23], v[228:231], v[148:151]\n\t"
        "ds_read_b128 v[248:251], v1 offset:32768\n\t"
        "v_mfma_f32_16x16x32_bf16 v[152:155], v[24:27], v[228:231], v[152:155]\n\t"
        "v_mfma_f32_16x16x32_bf16 v[156:159], v[20:23], v[232:235], v[156:159]\n\t"
        "ds_read_b128 v[228:231], v1 offset:23552\n\t"
        "v_mfma_f32_16x16x32_bf16 v[160:163], v[24:27], v[232:235], v[160:163]\n\t"
        "s_waitcnt lgkmcnt(3)\n\t"
        "v_mfma_f32_16x16x32_bf16 v[164:167], v[20:23], v[236:239], v[164:167]\n\t"
        "ds_read_b128 v[232:235], v1 offset:33792\n\t"
        "v_mfma_f32_16x16x32_bf16 v[168:171], v[24:27], v[236:239], v[168:171]\n\t"
        "v_mfma_f32_16x16x32_bf16 v[172:175], v[20:23], v[240:243], v[172:175]\n\t"
        "ds_read_b128 v[236:239], v1 offset:24576\n\t"
        "v_mfma_f32_16x16x32_bf16 v[176:179], v[24:27], v[240:243], v[176:179]\n\t"
        "s_waitcnt lgkmcnt(3)\n\t"
        "v_mfma_f32_16x16x32_bf16 v[180:183], v[20:23], v[244:247], v[180:183]\n\t"
        "ds_read_b128 v[240:243], v1 offset:34816\n\t"
        "v_mfma_f32_16x16x32_bf16 v[184:187], v[24:27], v[244:247], v[184:187]\n\t"
        "v_mfma_f32_16x16x32_bf16 v[188:191], v[20:23], v[248:251], v[188:191]\n\t"
        "ds_read_b128 v[244:247], v2 offset:20480\n\t"
        "v_mfma_f32_16x16x32_bf16 v[192:195], v[24:27], v[248:251], v[192:195]\n\t"
        "s_waitcnt lgkmcnt(3)\n\t"
        "v_mfma_f32_16x16x32_bf16 v[196:199], v[20:23], v[228:231], v[196:199]\n\t"
        "ds_read_b128 v[248:251], v2 offset:30720\n\t"
        "v_mfma_f32_16x16x32_bf16 v[200:203], v[24:27], v[228:231], v[200:203]\n\t"
        "v_mfma_f32_16x16x32_bf16 v[204:207], v[20:23], v[232:235], v[204:207]\n\t"
        "ds_read_b128 v[228:231], v2 offset:21504\n\t"
        "v_mfma_f32_16x16x32_bf16 v[208:211], v[24:27], v[232:235], v[208:211]\n\t"
        "s_waitcnt lgkmcnt(3)\n\t"
        "v_mfma_f32_16x16x32_bf16 v[212:215], v[20:23], v[236:239], v[212:215]\n\t"
        "ds_read_b128 v[232:235], v2 offset:31744\n\t"
        "v_mfma_f32_16x16x32_bf16 v[216:219], v[24:27], v[236:239], v[216:219]\n\t"
        "v_mfma_f32_16x16x32_bf16 v[220:223], v[20:23], v[240:243], v[220:223]\n\t"
        "ds_read_b128 v[236:239], v2 offset:22528\n\t"
        "v_mfma_f32_16x16x32_bf16 v[224:227], v[24:27], v[240:243], v[224:227]\n\t"
        "s_waitcnt lgkmcnt(3)\n\t"
        "v_mfma_f32_16x16x32_bf16 v[148:151], v[28:31], v[244:247], v[148:151]\n\t"
        "ds_read_b128 v[240:243], v2 offset:32768\n\t"
        "v_mfma_f32_16x16x32_bf16 v[152:155], v[32:35], v[244:247], v[152:155]\n\t"
        "v_mfma_f32_16x16x32_bf16 v[156:159], v[28:31], v[248:251], v[156:159]\n\t"
        "ds_read_b128 v[244:247], v2 offset:23552\n\t"
        "v_mfma_f32_16x16x32_bf16 v[160:163], v[32:35], v[248:251], v[160:163]\n\t"
        "s_waitcnt lgkmcnt(3)\n\t"
        "v_mfma_f32_16x16x32_bf16 v[164:167], v[28:31], v[228:231], v[164:167]\n\t"
        "ds_read_b128 v[248:251], v2 offset:33792\n\t"
        "v_mfma_f32_16x16x32_bf16 v[168:171], v[32:35], v[228:231], v[168:171]\n\t"
        "v_mfma_f32_16x16x32_bf16 v[172:175], v[28:31], v[232:235], v[172:175]\n\t"
        "ds_read_b128 v[228:231], v2 offset:24576\n\t"
        "v_mfma_f32_16x16x32_bf16 v[176:179], v[32:35], v[232:235], v[176:179]\n\t"
        "s_waitcnt lgkmcnt(3)\n\t"
        "v_mfma_f32_16x16x32_bf16 v[180:183], v[28:31], v[236:239], v[180:183]\n\t"
        "ds_read_b128 v[232:235], v2 offset:34816\n\t"
        "v_mfma_f32_16x16x32_bf16 v[184:187], v[32:35], v[236:239], v[184:187]\n\t"
        "v_mfma_f32_16x16x32_bf16 v[188:191], v[28:31], v[240:243], v[188:191]\n\t"
        "v_mfma_f32_16x16x32_bf16 v[192:195], v[32:35], v[240:243], v[192:195]\n\t"
        "s_waitcnt lgkmcnt(2)\n\t"
        "v_mfma_f32_16x16x32_bf16 v[196:199], v[28:31], v[244:247], v[196:199]\n\t"
        "v_mfma_f32_16x16x32_bf16 v[200:203], v[32:35], v[244:247], v[200:203]\n\t"
        "v_mfma_f32_16x16x32_bf16 v[204:207], v[28:31], v[248:251], v[204:207]\n\t"
        "v_mfma_f32_16x16x32_bf16 v[208:211], v[32:35], v[248:251], v[208:211]\n\t"
        "s_waitcnt lgkmcnt(0)\n\t"
        "v_mfma_f32_16x16x32_bf16 v[212:215], v[28:31], v[228:231], v[212:215]\n\t"
        "v_mfma_f32_16x16x32_bf16 v[216:219], v[32:35], v[228:231], v[216:219]\n\t"
        "v_mfma_f32_16x16x32_bf16 v[220:223], v[28:31], v[232:235], v[220:223]\n\t"
        "v_mfma_f32_16x16x32_bf16 v[224:227], v[32:35], v[232:235], v[224:227]\n\t"
        "s_cmp_eq_u32 s56, 0\n\t"
        "s_cselect_b32 s70, ((L_pit4_%=-L_begin_%=)&0xffffff80)+0, s70\n\t"
        "s_cmp_eq_u32 s56, 1\n\t"
        "s_cselect_b32 s70, ((L_cit4_%=-L_begin_%=)&0xffffff80)+0, s70\n\t"
        "s_cmp_eq_u32 s56, 2\n\t"
        "s_cselect_b32 s70, ((L_pit4_%=-L_begin_%=)&0xffffff80)+896, s70\n\t"
        "s_cmp_eq_u32 s56, 3\n\t"
        "s_cselect_b32 s70, ((L_cit5_%=-L_begin_%=)&0xffffff80)+0, s70\n\t"
        "buffer_load_dwordx4 v0, s[28:31], s70 offen lds\n\t"
        "L_cit2_%=:\n\t"
        "s_barrier\n\t"
        "ds_read_b128 v[36:39], v3 offset:0\n\t"
        "ds_read_b128 v[40:43], v3 offset:1024\n\t"
        "ds_read_b128 v[228:231], v1 offset:40960\n\t"
        "ds_read_b128 v[232:235], v1 offset:51200\n\t"
        "ds_read_b128 v[44:47], v3 offset:8192\n\t"
        "ds_read_b128 v[48:51], v3 offset:9216\n\t"
        "ds_read_b128 v[236:239], v1 offset:41984\n\t"
        "ds_read_b128 v[240:243], v1 offset:52224\n\t"
        "ds_read_b128 v[244:247], v1 offset:43008\n\t"
        "s_waitcnt lgkmcnt(5)\n\t"
        "v_mfma_f32_16x16x32_bf16 v[148:151], v[36:39], v[228:231], v[148:151]\n\t"
        "ds_read_b128 v[248:251], v1 offset:53248\n\t"
        "v_mfma_f32_16x16x32_bf16 v[152:155], v[40:43], v[228:231], v[152:155]\n\t"
        "v_mfma_f32_16x16x32_bf16 v[156:159], v[36:39], v[232:235], v[156:159]\n\t"
        "ds_read_b128 v[228:231], v1 offset:44032\n\t"
        "v_mfma_f32_16x16x32_bf16 v[160:163], v[40:43], v[232:235], v[160:163]\n\t"
        "s_waitcnt lgkmcnt(3)\n\t"
        "v_mfma_f32_16x16x32_bf16 v[164:167], v[36:39], v[236:239], v[164:167]\n\t"
        "ds_read_b128 v[232:235], v1 offset:54272\n\t"
        "v_mfma_f32_16x16x32_bf16 v[168:171], v[40:43], v[236:239], v[168:171]\n\t"
        "v_mfma_f32_16x16x32_bf16 v[172:175], v[36:39], v[240:243], v[172:175]\n\t"
        "ds_read_b128 v[236:239], v1 offset:45056\n\t"
        "v_mfma_f32_16x16x32_bf16 v[176:179], v[40:43], v[240:243], v[176:179]\n\t"
        "s_waitcnt lgkmcnt(3)\n\t"
        "v_mfma_f32_16x16x32_bf16 v[180:183], v[36:39], v[244:247], v[180:183]\n\t"
        "ds_read_b128 v[240:243], v1 offset:55296\n\t"
        "v_mfma_f32_16x16x32_bf16 v[184:187], v[40:43], v[244:247], v[184:187]\n\t"
        "v_mfma_f32_16x16x32_bf16 v[188:191], v[36:39], v[248:251], v[188:191]\n\t"
        "ds_read_b128 v[244:247], v2 offset:40960\n\t"
        "v_mfma_f32_16x16x32_bf16 v[192:195], v[40:43], v[248:251], v[192:195]\n\t"
        "s_waitcnt lgkmcnt(3)\n\t"
        "v_mfma_f32_16x16x32_bf16 v[196:199], v[36:39], v[228:231], v[196:199]\n\t"
        "ds_read_b128 v[248:251], v2 offset:51200\n\t"
        "v_mfma_f32_16x16x32_bf16 v[200:203], v[40:43], v[228:231], v[200:203]\n\t"
        "v_mfma_f32_16x16x32_bf16 v[204:207], v[36:39], v[232:235], v[204:207]\n\t"
        "ds_read_b128 v[228:231], v2 offset:41984\n\t"
        "v_mfma_f32_16x16x32_bf16 v[208:211], v[40:43], v[232:235], v[208:211]\n\t"
        "s_waitcnt lgkmcnt(3)\n\t"
        "v_mfma_f32_16x16x32_bf16 v[212:215], v[36:39], v[236:239], v[212:215]\n\t"
        "ds_read_b128 v[232:235], v2 offset:52224\n\t"
        "v_mfma_f32_16x16x32_bf16 v[216:219], v[40:43], v[236:239], v[216:219]\n\t"
        "v_mfma_f32_16x16x32_bf16 v[220:223], v[36:39], v[240:243], v[220:223]\n\t"
        "ds_read_b128 v[236:239], v2 offset:43008\n\t"
        "v_mfma_f32_16x16x32_bf16 v[224:227], v[40:43], v[240:243], v[224:227]\n\t"
        "s_waitcnt lgkmcnt(3)\n\t"
        "v_mfma_f32_16x16x32_bf16 v[148:151], v[44:47], v[244:247], v[148:151]\n\t"
        "ds_read_b128 v[240:243], v2 offset:53248\n\t"
        "v_mfma_f32_16x16x32_bf16 v[152:155], v[48:51], v[244:247], v[152:155]\n\t"
        "v_mfma_f32_16x16x32_bf16 v[156:159], v[44:47], v[248:251], v[156:159]\n\t"
        "ds_read_b128 v[244:247], v2 offset:44032\n\t"
        "v_mfma_f32_16x16x32_bf16 v[160:163], v[48:51], v[248:251], v[160:163]\n\t"
        "s_waitcnt lgkmcnt(3)\n\t"
        "v_mfma_f32_16x16x32_bf16 v[164:167], v[44:47], v[228:231], v[164:167]\n\t"
        "ds_read_b128 v[248:251], v2 offset:54272\n\t"
        "v_mfma_f32_16x16x32_bf16 v[168:171], v[48:51], v[228:231], v[168:171]\n\t"
        "v_mfma_f32_16x16x32_bf16 v[172:175], v[44:47], v[232:235], v[172:175]\n\t"
        "ds_read_b128 v[228:231], v2 offset:45056\n\t"
        "v_mfma_f32_16x16x32_bf16 v[176:179], v[48:51], v[232:235], v[176:179]\n\t"
        "s_waitcnt lgkmcnt(3)\n\t"
        "v_mfma_f32_16x16x32_bf16 v[180:183], v[44:47], v[236:239], v[180:183]\n\t"
        "ds_read_b128 v[232:235], v2 offset:55296\n\t"
        "v_mfma_f32_16x16x32_bf16 v[184:187], v[48:51], v[236:239], v[184:187]\n\t"
        "v_mfma_f32_16x16x32_bf16 v[188:191], v[44:47], v[240:243], v[188:191]\n\t"
        "v_mfma_f32_16x16x32_bf16 v[192:195], v[48:51], v[240:243], v[192:195]\n\t"
        "s_waitcnt lgkmcnt(2)\n\t"
        "v_mfma_f32_16x16x32_bf16 v[196:199], v[44:47], v[244:247], v[196:199]\n\t"
        "v_mfma_f32_16x16x32_bf16 v[200:203], v[48:51], v[244:247], v[200:203]\n\t"
        "v_mfma_f32_16x16x32_bf16 v[204:207], v[44:47], v[248:251], v[204:207]\n\t"
        "v_mfma_f32_16x16x32_bf16 v[208:211], v[48:51], v[248:251], v[208:211]\n\t"
        "s_waitcnt lgkmcnt(0)\n\t"
        "v_mfma_f32_16x16x32_bf16 v[212:215], v[44:47], v[228:231], v[212:215]\n\t"
        "v_mfma_f32_16x16x32_bf16 v[216:219], v[48:51], v[228:231], v[216:219]\n\t"
        "v_mfma_f32_16x16x32_bf16 v[220:223], v[44:47], v[232:235], v[220:223]\n\t"
        "v_mfma_f32_16x16x32_bf16 v[224:227], v[48:51], v[232:235], v[224:227]\n\t"
        "s_cmp_eq_u32 s56, 0\n\t"
        "s_cselect_b32 s70, ((L_pit5_%=-L_begin_%=)&0xffffff80)+0, s70\n\t"
        "s_cmp_eq_u32 s56, 1\n\t"
        "s_cselect_b32 s70, ((L_cit5_%=-L_begin_%=)&0xffffff80)+0, s70\n\t"
        "s_cmp_eq_u32 s56, 2\n\t"
        "s_cselect_b32 s70, ((L_pit5_%=-L_begin_%=)&0xffffff80)+896, s70\n\t"
        "s_cmp_eq_u32 s56, 3\n\t"
        "s_cselect_b32 s70, ((L_cit6_%=-L_begin_%=)&0xffffff80)+0, s70\n\t"
        "buffer_load_dwordx4 v0, s[28:31], s70 offen lds\n\t"
        "L_cit3_%=:\n\t"
        "s_barrier\n\t"
        "ds_read_b128 v[52:55], v3 offset:16384\n\t"
        "ds_read_b128 v[56:59], v3 offset:17408\n\t"
        "ds_read_b128 v[228:231], v1 offset:0\n\t"
        "ds_read_b128 v[232:235], v1 offset:10240\n\t"
        "ds_read_b128 v[60:63], v3 offset:24576\n\t"
        "ds_read_b128 v[64:67], v3 offset:25600\n\t"
        "ds_read_b128 v[236:239], v1 offset:1024\n\t"
        "ds_read_b128 v[240:243], v1 offset:11264\n\t"
        "ds_read_b128 v[244:247], v1 offset:2048\n\t"
        "s_waitcnt lgkmcnt(5)\n\t"
        "v_mfma_f32_16x16x32_bf16 v[148:151], v[52:55], v[228:231], v[148:151]\n\t"
        "ds_read_b128 v[248:251], v1 offset:12288\n\t"
        "v_mfma_f32_16x16x32_bf16 v[152:155], v[56:59], v[228:231], v[152:155]\n\t"
        "v_mfma_f32_16x16x32_bf16 v[156:159], v[52:55], v[232:235], v[156:159]\n\t"
        "ds_read_b128 v[228:231], v1 offset:3072\n\t"
        "v_mfma_f32_16x16x32_bf16 v[160:163], v[56:59], v[232:235], v[160:163]\n\t"
        "s_waitcnt lgkmcnt(3)\n\t"
        "v_mfma_f32_16x16x32_bf16 v[164:167], v[52:55], v[236:239], v[164:167]\n\t"
        "ds_read_b128 v[232:235], v1 offset:13312\n\t"
        "v_mfma_f32_16x16x32_bf16 v[168:171], v[56:59], v[236:239], v[168:171]\n\t"
        "v_mfma_f32_16x16x32_bf16 v[172:175], v[52:55], v[240:243], v[172:175]\n\t"
        "ds_read_b128 v[236:239], v1 offset:4096\n\t"
        "v_mfma_f32_16x16x32_bf16 v[176:179], v[56:59], v[240:243], v[176:179]\n\t"
        "s_waitcnt lgkmcnt(3)\n\t"
        "v_mfma_f32_16x16x32_bf16 v[180:183], v[52:55], v[244:247], v[180:183]\n\t"
        "ds_read_b128 v[240:243], v1 offset:14336\n\t"
        "v_mfma_f32_16x16x32_bf16 v[184:187], v[56:59], v[244:247], v[184:187]\n\t"
        "v_mfma_f32_16x16x32_bf16 v[188:191], v[52:55], v[248:251], v[188:191]\n\t"
        "ds_read_b128 v[244:247], v2 offset:0\n\t"
        "v_mfma_f32_16x16x32_bf16 v[192:195], v[56:59], v[248:251], v[192:195]\n\t"
        "s_waitcnt lgkmcnt(3)\n\t"
        "v_mfma_f32_16x16x32_bf16 v[196:199], v[52:55], v[228:231], v[196:199]\n\t"
        "ds_read_b128 v[248:251], v2 offset:10240\n\t"
        "v_mfma_f32_16x16x32_bf16 v[200:203], v[56:59], v[228:231], v[200:203]\n\t"
        "v_mfma_f32_16x16x32_bf16 v[204:207], v[52:55], v[232:235], v[204:207]\n\t"
        "ds_read_b128 v[228:231], v2 offset:1024\n\t"
        "v_mfma_f32_16x16x32_bf16 v[208:211], v[56:59], v[232:235], v[208:211]\n\t"
        "s_waitcnt lgkmcnt(3)\n\t"
        "v_mfma_f32_16x16x32_bf16 v[212:215], v[52:55], v[236:239], v[212:215]\n\t"
        "ds_read_b128 v[232:235], v2 offset:11264\n\t"
        "v_mfma_f32_16x16x32_bf16 v[216:219], v[56:59], v[236:239], v[216:219]\n\t"
        "v_mfma_f32_16x16x32_bf16 v[220:223], v[52:55], v[240:243], v[220:223]\n\t"
        "ds_read_b128 v[236:239], v2 offset:2048\n\t"
        "v_mfma_f32_16x16x32_bf16 v[224:227], v[56:59], v[240:243], v[224:227]\n\t"
        "s_waitcnt lgkmcnt(3)\n\t"
        "v_mfma_f32_16x16x32_bf16 v[148:151], v[60:63], v[244:247], v[148:151]\n\t"
        "ds_read_b128 v[240:243], v2 offset:12288\n\t"
        "v_mfma_f32_16x16x32_bf16 v[152:155], v[64:67], v[244:247], v[152:155]\n\t"
        "v_mfma_f32_16x16x32_bf16 v[156:159], v[60:63], v[248:251], v[156:159]\n\t"
        "ds_read_b128 v[244:247], v2 offset:3072\n\t"
        "v_mfma_f32_16x16x32_bf16 v[160:163], v[64:67], v[248:251], v[160:163]\n\t"
        "s_waitcnt lgkmcnt(3)\n\t"
        "v_mfma_f32_16x16x32_bf16 v[164:167], v[60:63], v[228:231], v[164:167]\n\t"
        "ds_read_b128 v[248:251], v2 offset:13312\n\t"
        "v_mfma_f32_16x16x32_bf16 v[168:171], v[64:67], v[228:231], v[168:171]\n\t"
        "v_mfma_f32_16x16x32_bf16 v[172:175], v[60:63], v[232:235], v[172:175]\n\t"
        "ds_read_b128 v[228:231], v2 offset:4096\n\t"
        "v_mfma_f32_16x16x32_bf16 v[176:179], v[64:67], v[232:235], v[176:179]\n\t"
        "s_waitcnt lgkmcnt(3)\n\t"
        "v_mfma_f32_16x16x32_bf16 v[180:183], v[60:63], v[236:239], v[180:183]\n\t"
        "ds_read_b128 v[232:235], v2 offset:14336\n\t"
        "v_mfma_f32_16x16x32_bf16 v[184:187], v[64:67], v[236:239], v[184:187]\n\t"
        "v_mfma_f32_16x16x32_bf16 v[188:191], v[60:63], v[240:243], v[188:191]\n\t"
        "v_mfma_f32_16x16x32_bf16 v[192:195], v[64:67], v[240:243], v[192:195]\n\t"
        "s_waitcnt lgkmcnt(2)\n\t"
        "v_mfma_f32_16x16x32_bf16 v[196:199], v[60:63], v[244:247], v[196:199]\n\t"
        "v_mfma_f32_16x16x32_bf16 v[200:203], v[64:67], v[244:247], v[200:203]\n\t"
        "v_mfma_f32_16x16x32_bf16 v[204:207], v[60:63], v[248:251], v[204:207]\n\t"
        "v_mfma_f32_16x16x32_bf16 v[208:211], v[64:67], v[248:251], v[208:211]\n\t"
        "s_waitcnt lgkmcnt(0)\n\t"
        "v_mfma_f32_16x16x32_bf16 v[212:215], v[60:63], v[228:231], v[212:215]\n\t"
        "v_mfma_f32_16x16x32_bf16 v[216:219], v[64:67], v[228:231], v[216:219]\n\t"
        "v_mfma_f32_16x16x32_bf16 v[220:223], v[60:63], v[232:235], v[220:223]\n\t"
        "v_mfma_f32_16x16x32_bf16 v[224:227], v[64:67], v[232:235], v[224:227]\n\t"
        "s_cmp_eq_u32 s56, 0\n\t"
        "s_cselect_b32 s70, ((L_pit6_%=-L_begin_%=)&0xffffff80)+0, s70\n\t"
        "s_cmp_eq_u32 s56, 1\n\t"
        "s_cselect_b32 s70, ((L_cit6_%=-L_begin_%=)&0xffffff80)+0, s70\n\t"
        "s_cmp_eq_u32 s56, 2\n\t"
        "s_cselect_b32 s70, ((L_pit6_%=-L_begin_%=)&0xffffff80)+896, s70\n\t"
        "s_cmp_eq_u32 s56, 3\n\t"
        "s_cselect_b32 s70, ((L_cit7_%=-L_begin_%=)&0xffffff80)+0, s70\n\t"
        "buffer_load_dwordx4 v0, s[28:31], s70 offen lds\n\t"
        "L_cit4_%=:\n\t"
        "s_barrier\n\t"
        "ds_read_b128 v[68:71], v3 offset:0\n\t"
        "ds_read_b128 v[72:75], v3 offset:1024\n\t"
        "ds_read_b128 v[228:231], v1 offset:20480\n\t"
        "ds_read_b128 v[232:235], v1 offset:30720\n\t"
        "ds_read_b128 v[76:79], v3 offset:8192\n\t"
        "ds_read_b128 v[80:83], v3 offset:9216\n\t"
        "ds_read_b128 v[236:239], v1 offset:21504\n\t"
        "ds_read_b128 v[240:243], v1 offset:31744\n\t"
        "ds_read_b128 v[244:247], v1 offset:22528\n\t"
        "s_waitcnt lgkmcnt(5)\n\t"
        "v_mfma_f32_16x16x32_bf16 v[148:151], v[68:71], v[228:231], v[148:151]\n\t"
        "ds_read_b128 v[248:251], v1 offset:32768\n\t"
        "v_mfma_f32_16x16x32_bf16 v[152:155], v[72:75], v[228:231], v[152:155]\n\t"
        "v_mfma_f32_16x16x32_bf16 v[156:159], v[68:71], v[232:235], v[156:159]\n\t"
        "ds_read_b128 v[228:231], v1 offset:23552\n\t"
        "v_mfma_f32_16x16x32_bf16 v[160:163], v[72:75], v[232:235], v[160:163]\n\t"
        "s_waitcnt lgkmcnt(3)\n\t"
        "v_mfma_f32_16x16x32_bf16 v[164:167], v[68:71], v[236:239], v[164:167]\n\t"
        "ds_read_b128 v[232:235], v1 offset:33792\n\t"
        "v_mfma_f32_16x16x32_bf16 v[168:171], v[72:75], v[236:239], v[168:171]\n\t"
        "v_mfma_f32_16x16x32_bf16 v[172:175], v[68:71], v[240:243], v[172:175]\n\t"
        "ds_read_b128 v[236:239], v1 offset:24576\n\t"
        "v_mfma_f32_16x16x32_bf16 v[176:179], v[72:75], v[240:243], v[176:179]\n\t"
        "s_waitcnt lgkmcnt(3)\n\t"
        "v_mfma_f32_16x16x32_bf16 v[180:183], v[68:71], v[244:247], v[180:183]\n\t"
        "ds_read_b128 v[240:243], v1 offset:34816\n\t"
        "v_mfma_f32_16x16x32_bf16 v[184:187], v[72:75], v[244:247], v[184:187]\n\t"
        "v_mfma_f32_16x16x32_bf16 v[188:191], v[68:71], v[248:251], v[188:191]\n\t"
        "ds_read_b128 v[244:247], v2 offset:20480\n\t"
        "v_mfma_f32_16x16x32_bf16 v[192:195], v[72:75], v[248:251], v[192:195]\n\t"
        "s_waitcnt lgkmcnt(3)\n\t"
        "v_mfma_f32_16x16x32_bf16 v[196:199], v[68:71], v[228:231], v[196:199]\n\t"
        "ds_read_b128 v[248:251], v2 offset:30720\n\t"
        "v_mfma_f32_16x16x32_bf16 v[200:203], v[72:75], v[228:231], v[200:203]\n\t"
        "v_mfma_f32_16x16x32_bf16 v[204:207], v[68:71], v[232:235], v[204:207]\n\t"
        "ds_read_b128 v[228:231], v2 offset:21504\n\t"
        "v_mfma_f32_16x16x32_bf16 v[208:211], v[72:75], v[232:235], v[208:211]\n\t"
        "s_waitcnt lgkmcnt(3)\n\t"
        "v_mfma_f32_16x16x32_bf16 v[212:215], v[68:71], v[236:239], v[212:215]\n\t"
        "ds_read_b128 v[232:235], v2 offset:31744\n\t"
        "v_mfma_f32_16x16x32_bf16 v[216:219], v[72:75], v[236:239], v[216:219]\n\t"
        "v_mfma_f32_16x16x32_bf16 v[220:223], v[68:71], v[240:243], v[220:223]\n\t"
        "ds_read_b128 v[236:239], v2 offset:22528\n\t"
        "v_mfma_f32_16x16x32_bf16 v[224:227], v[72:75], v[240:243], v[224:227]\n\t"
        "s_waitcnt lgkmcnt(3)\n\t"
        "v_mfma_f32_16x16x32_bf16 v[148:151], v[76:79], v[244:247], v[148:151]\n\t"
        "ds_read_b128 v[240:243], v2 offset:32768\n\t"
        "v_mfma_f32_16x16x32_bf16 v[152:155], v[80:83], v[244:247], v[152:155]\n\t"
        "v_mfma_f32_16x16x32_bf16 v[156:159], v[76:79], v[248:251], v[156:159]\n\t"
        "ds_read_b128 v[244:247], v2 offset:23552\n\t"
        "v_mfma_f32_16x16x32_bf16 v[160:163], v[80:83], v[248:251], v[160:163]\n\t"
        "s_waitcnt lgkmcnt(3)\n\t"
        "v_mfma_f32_16x16x32_bf16 v[164:167], v[76:79], v[228:231], v[164:167]\n\t"
        "ds_read_b128 v[248:251], v2 offset:33792\n\t"
        "v_mfma_f32_16x16x32_bf16 v[168:171], v[80:83], v[228:231], v[168:171]\n\t"
        "v_mfma_f32_16x16x32_bf16 v[172:175], v[76:79], v[232:235], v[172:175]\n\t"
        "ds_read_b128 v[228:231], v2 offset:24576\n\t"
        "v_mfma_f32_16x16x32_bf16 v[176:179], v[80:83], v[232:235], v[176:179]\n\t"
        "s_waitcnt lgkmcnt(3)\n\t"
        "v_mfma_f32_16x16x32_bf16 v[180:183], v[76:79], v[236:239], v[180:183]\n\t"
        "ds_read_b128 v[232:235], v2 offset:34816\n\t"
        "v_mfma_f32_16x16x32_bf16 v[184:187], v[80:83], v[236:239], v[184:187]\n\t"
        "v_mfma_f32_16x16x32_bf16 v[188:191], v[76:79], v[240:243], v[188:191]\n\t"
        "v_mfma_f32_16x16x32_bf16 v[192:195], v[80:83], v[240:243], v[192:195]\n\t"
        "s_waitcnt lgkmcnt(2)\n\t"
        "v_mfma_f32_16x16x32_bf16 v[196:199], v[76:79], v[244:247], v[196:199]\n\t"
        "v_mfma_f32_16x16x32_bf16 v[200:203], v[80:83], v[244:247], v[200:203]\n\t"
        "v_mfma_f32_16x16x32_bf16 v[204:207], v[76:79], v[248:251], v[204:207]\n\t"
        "v_mfma_f32_16x16x32_bf16 v[208:211], v[80:83], v[248:251], v[208:211]\n\t"
        "s_waitcnt lgkmcnt(0)\n\t"
        "v_mfma_f32_16x16x32_bf16 v[212:215], v[76:79], v[228:231], v[212:215]\n\t"
        "v_mfma_f32_16x16x32_bf16 v[216:219], v[80:83], v[228:231], v[216:219]\n\t"
        "v_mfma_f32_16x16x32_bf16 v[220:223], v[76:79], v[232:235], v[220:223]\n\t"
        "v_mfma_f32_16x16x32_bf16 v[224:227], v[80:83], v[232:235], v[224:227]\n\t"
        "s_cmp_eq_u32 s56, 0\n\t"
        "s_cselect_b32 s70, ((L_pit7_%=-L_begin_%=)&0xffffff80)+0, s70\n\t"
        "s_cmp_eq_u32 s56, 1\n\t"
        "s_cselect_b32 s70, ((L_cit7_%=-L_begin_%=)&0xffffff80)+0, s70\n\t"
        "s_cmp_eq_u32 s56, 2\n\t"
        "s_cselect_b32 s70, ((L_pit7_%=-L_begin_%=)&0xffffff80)+896, s70\n\t"
        "s_cmp_eq_u32 s56, 3\n\t"
        "s_cselect_b32 s70, ((L_cit8_%=-L_begin_%=)&0xffffff80)+0, s70\n\t"
        "buffer_load_dwordx4 v0, s[28:31], s70 offen lds\n\t"
        "s_cmp_eq_u32 s56, 0\n\t"
        "s_cselect_b32 s70, ((L_ppost_%=-L_begin_%=)&0xffffff80)+0, s70\n\t"
        "s_cmp_eq_u32 s56, 1\n\t"
        "s_cselect_b32 s70, ((L_ppost_%=-L_begin_%=)&0xffffff80)+1024, s70\n\t"
        "s_cmp_eq_u32 s56, 2\n\t"
        "s_cselect_b32 s70, ((L_st_%=-L_begin_%=)&0xffffff80)+0, s70\n\t"
        "s_cmp_eq_u32 s56, 3\n\t"
        "s_cselect_b32 s70, ((L_st_%=-L_begin_%=)&0xffffff80)+1024, s70\n\t"
        "buffer_load_dwordx4 v0, s[28:31], s70 offen lds\n\t"
        "L_cit5_%=:\n\t"
        "s_barrier\n\t"
        "ds_read_b128 v[84:87], v3 offset:16384\n\t"
        "ds_read_b128 v[88:91], v3 offset:17408\n\t"
        "ds_read_b128 v[228:231], v1 offset:40960\n\t"
        "ds_read_b128 v[232:235], v1 offset:51200\n\t"
        "ds_read_b128 v[92:95], v3 offset:24576\n\t"
        "ds_read_b128 v[96:99], v3 offset:25600\n\t"
        "ds_read_b128 v[236:239], v1 offset:41984\n\t"
        "ds_read_b128 v[240:243], v1 offset:52224\n\t"
        "ds_read_b128 v[244:247], v1 offset:43008\n\t"
        "s_waitcnt lgkmcnt(5)\n\t"
        "v_mfma_f32_16x16x32_bf16 v[148:151], v[84:87], v[228:231], v[148:151]\n\t"
        "ds_read_b128 v[248:251], v1 offset:53248\n\t"
        "v_mfma_f32_16x16x32_bf16 v[152:155], v[88:91], v[228:231], v[152:155]\n\t"
        "v_mfma_f32_16x16x32_bf16 v[156:159], v[84:87], v[232:235], v[156:159]\n\t"
        "ds_read_b128 v[228:231], v1 offset:44032\n\t"
        "v_mfma_f32_16x16x32_bf16 v[160:163], v[88:91], v[232:235], v[160:163]\n\t"
        "s_waitcnt lgkmcnt(3)\n\t"
        "v_mfma_f32_16x16x32_bf16 v[164:167], v[84:87], v[236:239], v[164:167]\n\t"
        "ds_read_b128 v[232:235], v1 offset:54272\n\t"
        "v_mfma_f32_16x16x32_bf16 v[168:171], v[88:91], v[236:239], v[168:171]\n\t"
        "v_mfma_f32_16x16x32_bf16 v[172:175], v[84:87], v[240:243], v[172:175]\n\t"
        "ds_read_b128 v[236:239], v1 offset:45056\n\t"
        "v_mfma_f32_16x16x32_bf16 v[176:179], v[88:91], v[240:243], v[176:179]\n\t"
        "s_waitcnt lgkmcnt(3)\n\t"
        "v_mfma_f32_16x16x32_bf16 v[180:183], v[84:87], v[244:247], v[180:183]\n\t"
        "ds_read_b128 v[240:243], v1 offset:55296\n\t"
        "v_mfma_f32_16x16x32_bf16 v[184:187], v[88:91], v[244:247], v[184:187]\n\t"
        "v_mfma_f32_16x16x32_bf16 v[188:191], v[84:87], v[248:251], v[188:191]\n\t"
        "ds_read_b128 v[244:247], v2 offset:40960\n\t"
        "v_mfma_f32_16x16x32_bf16 v[192:195], v[88:91], v[248:251], v[192:195]\n\t"
        "s_waitcnt lgkmcnt(3)\n\t"
        "v_mfma_f32_16x16x32_bf16 v[196:199], v[84:87], v[228:231], v[196:199]\n\t"
        "ds_read_b128 v[248:251], v2 offset:51200\n\t"
        "v_mfma_f32_16x16x32_bf16 v[200:203], v[88:91], v[228:231], v[200:203]\n\t"
        "v_mfma_f32_16x16x32_bf16 v[204:207], v[84:87], v[232:235], v[204:207]\n\t"
        "ds_read_b128 v[228:231], v2 offset:41984\n\t"
        "v_mfma_f32_16x16x32_bf16 v[208:211], v[88:91], v[232:235], v[208:211]\n\t"
        "s_waitcnt lgkmcnt(3)\n\t"
        "v_mfma_f32_16x16x32_bf16 v[212:215], v[84:87], v[236:239], v[212:215]\n\t"
        "ds_read_b128 v[232:235], v2 offset:52224\n\t"
        "v_mfma_f32_16x16x32_bf16 v[216:219], v[88:91], v[236:239], v[216:219]\n\t"
        "v_mfma_f32_16x16x32_bf16 v[220:223], v[84:87], v[240:243], v[220:223]\n\t"
        "ds_read_b128 v[236:239], v2 offset:43008\n\t"
        "v_mfma_f32_16x16x32_bf16 v[224:227], v[88:91], v[240:243], v[224:227]\n\t"
        "s_waitcnt lgkmcnt(3)\n\t"
        "v_mfma_f32_16x16x32_bf16 v[148:151], v[92:95], v[244:247], v[148:151]\n\t"
        "ds_read_b128 v[240:243], v2 offset:53248\n\t"
        "v_mfma_f32_16x16x32_bf16 v[152:155], v[96:99], v[244:247], v[152:155]\n\t"
        "v_mfma_f32_16x16x32_bf16 v[156:159], v[92:95], v[248:251], v[156:159]\n\t"
        "ds_read_b128 v[244:247], v2 offset:44032\n\t"
        "v_mfma_f32_16x16x32_bf16 v[160:163], v[96:99], v[248:251], v[160:163]\n\t"
        "s_waitcnt lgkmcnt(3)\n\t"
        "v_mfma_f32_16x16x32_bf16 v[164:167], v[92:95], v[228:231], v[164:167]\n\t"
        "ds_read_b128 v[248:251], v2 offset:54272\n\t"
        "v_mfma_f32_16x16x32_bf16 v[168:171], v[96:99], v[228:231], v[168:171]\n\t"
        "v_mfma_f32_16x16x32_bf16 v[172:175], v[92:95], v[232:235], v[172:175]\n\t"
        "ds_read_b128 v[228:231], v2 offset:45056\n\t"
        "v_mfma_f32_16x16x32_bf16 v[176:179], v[96:99], v[232:235], v[176:179]\n\t"
        "s_waitcnt lgkmcnt(3)\n\t"
        "v_mfma_f32_16x16x32_bf16 v[180:183], v[92:95], v[236:239], v[180:183]\n\t"
        "ds_read_b128 v[232:235], v2 offset:55296\n\t"
        "v_mfma_f32_16x16x32_bf16 v[184:187], v[96:99], v[236:239], v[184:187]\n\t"
        "v_mfma_f32_16x16x32_bf16 v[188:191], v[92:95], v[240:243], v[188:191]\n\t"
        "v_mfma_f32_16x16x32_bf16 v[192:195], v[96:99], v[240:243], v[192:195]\n\t"
        "s_waitcnt lgkmcnt(2)\n\t"
        "v_mfma_f32_16x16x32_bf16 v[196:199], v[92:95], v[244:247], v[196:199]\n\t"
        "v_mfma_f32_16x16x32_bf16 v[200:203], v[96:99], v[244:247], v[200:203]\n\t"
        "v_mfma_f32_16x16x32_bf16 v[204:207], v[92:95], v[248:251], v[204:207]\n\t"
        "v_mfma_f32_16x16x32_bf16 v[208:211], v[96:99], v[248:251], v[208:211]\n\t"
        "s_waitcnt lgkmcnt(0)\n\t"
        "v_mfma_f32_16x16x32_bf16 v[212:215], v[92:95], v[228:231], v[212:215]\n\t"
        "v_mfma_f32_16x16x32_bf16 v[216:219], v[96:99], v[228:231], v[216:219]\n\t"
        "v_mfma_f32_16x16x32_bf16 v[220:223], v[92:95], v[232:235], v[220:223]\n\t"
        "v_mfma_f32_16x16x32_bf16 v[224:227], v[96:99], v[232:235], v[224:227]\n\t"
        "s_cmp_eq_u32 s56, 0\n\t"
        "s_cselect_b32 s70, ((L_cp2_%=-L_begin_%=)&0xffffff80)+0, s70\n\t"
        "s_cmp_eq_u32 s56, 1\n\t"
        "s_cselect_b32 s70, ((L_cp2_%=-L_begin_%=)&0xffffff80)+1024, s70\n\t"
        "s_cmp_eq_u32 s56, 2\n\t"
        "s_cselect_b32 s70, ((L_cp2_%=-L_begin_%=)&0xffffff80)+2048, s70\n\t"
        "s_cmp_eq_u32 s56, 3\n\t"
        "s_cselect_b32 s70, ((L_cp2_%=-L_begin_%=)&0xffffff80)+3072, s70\n\t"
        "buffer_load_dwordx4 v0, s[28:31], s70 offen lds\n\t"
        "L_cit6_%=:\n\t"
        "s_barrier\n\t"
        "ds_read_b128 v[100:103], v3 offset:0\n\t"
        "ds_read_b128 v[104:107], v3 offset:1024\n\t"
        "ds_read_b128 v[228:231], v1 offset:0\n\t"
        "ds_read_b128 v[232:235], v1 offset:10240\n\t"
        "ds_read_b128 v[108:111], v3 offset:8192\n\t"
        "ds_read_b128 v[112:115], v3 offset:9216\n\t"
        "ds_read_b128 v[236:239], v1 offset:1024\n\t"
        "ds_read_b128 v[240:243], v1 offset:11264\n\t"
        "ds_read_b128 v[244:247], v1 offset:2048\n\t"
        "s_waitcnt lgkmcnt(5)\n\t"
        "v_mfma_f32_16x16x32_bf16 v[148:151], v[100:103], v[228:231], v[148:151]\n\t"
        "ds_read_b128 v[248:251], v1 offset:12288\n\t"
        "v_mfma_f32_16x16x32_bf16 v[152:155], v[104:107], v[228:231], v[152:155]\n\t"
        "v_mfma_f32_16x16x32_bf16 v[156:159], v[100:103], v[232:235], v[156:159]\n\t"
        "ds_read_b128 v[228:231], v1 offset:3072\n\t"
        "v_mfma_f32_16x16x32_bf16 v[160:163], v[104:107], v[232:235], v[160:163]\n\t"
        "s_waitcnt lgkmcnt(3)\n\t"
        "v_mfma_f32_16x16x32_bf16 v[164:167], v[100:103], v[236:239], v[164:167]\n\t"
        "ds_read_b128 v[232:235], v1 offset:13312\n\t"
        "v_mfma_f32_16x16x32_bf16 v[168:171], v[104:107], v[236:239], v[168:171]\n\t"
        "v_mfma_f32_16x16x32_bf16 v[172:175], v[100:103], v[240:243], v[172:175]\n\t"
        "ds_read_b128 v[236:239], v1 offset:4096\n\t"
        "v_mfma_f32_16x16x32_bf16 v[176:179], v[104:107], v[240:243], v[176:179]\n\t"
        "s_waitcnt lgkmcnt(3)\n\t"
        "v_mfma_f32_16x16x32_bf16 v[180:183], v[100:103], v[244:247], v[180:183]\n\t"
        "ds_read_b128 v[240:243], v1 offset:14336\n\t"
        "v_mfma_f32_16x16x32_bf16 v[184:187], v[104:107], v[244:247], v[184:187]\n\t"
        "v_mfma_f32_16x16x32_bf16 v[188:191], v[100:103], v[248:251], v[188:191]\n\t"
        "ds_read_b128 v[244:247], v2 offset:0\n\t"
        "v_mfma_f32_16x16x32_bf16 v[192:195], v[104:107], v[248:251], v[192:195]\n\t"
        "s_waitcnt lgkmcnt(3)\n\t"
        "v_mfma_f32_16x16x32_bf16 v[196:199], v[100:103], v[228:231], v[196:199]\n\t"
        "ds_read_b128 v[248:251], v2 offset:10240\n\t"
        "v_mfma_f32_16x16x32_bf16 v[200:203], v[104:107], v[228:231], v[200:203]\n\t"
        "v_mfma_f32_16x16x32_bf16 v[204:207], v[100:103], v[232:235], v[204:207]\n\t"
        "ds_read_b128 v[228:231], v2 offset:1024\n\t"
        "v_mfma_f32_16x16x32_bf16 v[208:211], v[104:107], v[232:235], v[208:211]\n\t"
        "s_waitcnt lgkmcnt(3)\n\t"
        "v_mfma_f32_16x16x32_bf16 v[212:215], v[100:103], v[236:239], v[212:215]\n\t"
        "ds_read_b128 v[232:235], v2 offset:11264\n\t"
        "v_mfma_f32_16x16x32_bf16 v[216:219], v[104:107], v[236:239], v[216:219]\n\t"
        "v_mfma_f32_16x16x32_bf16 v[220:223], v[100:103], v[240:243], v[220:223]\n\t"
        "ds_read_b128 v[236:239], v2 offset:2048\n\t"
        "v_mfma_f32_16x16x32_bf16 v[224:227], v[104:107], v[240:243], v[224:227]\n\t"
        "s_waitcnt lgkmcnt(3)\n\t"
        "v_mfma_f32_16x16x32_bf16 v[148:151], v[108:111], v[244:247], v[148:151]\n\t"
        "ds_read_b128 v[240:243], v2 offset:12288\n\t"
        "v_mfma_f32_16x16x32_bf16 v[152:155], v[112:115], v[244:247], v[152:155]\n\t"
        "v_mfma_f32_16x16x32_bf16 v[156:159], v[108:111], v[248:251], v[156:159]\n\t"
        "ds_read_b128 v[244:247], v2 offset:3072\n\t"
        "v_mfma_f32_16x16x32_bf16 v[160:163], v[112:115], v[248:251], v[160:163]\n\t"
        "s_waitcnt lgkmcnt(3)\n\t"
        "v_mfma_f32_16x16x32_bf16 v[164:167], v[108:111], v[228:231], v[164:167]\n\t"
        "ds_read_b128 v[248:251], v2 offset:13312\n\t"
        "v_mfma_f32_16x16x32_bf16 v[168:171], v[112:115], v[228:231], v[168:171]\n\t"
        "v_mfma_f32_16x16x32_bf16 v[172:175], v[108:111], v[232:235], v[172:175]\n\t"
        "ds_read_b128 v[228:231], v2 offset:4096\n\t"
        "v_mfma_f32_16x16x32_bf16 v[176:179], v[112:115], v[232:235], v[176:179]\n\t"
        "s_waitcnt lgkmcnt(3)\n\t"
        "v_mfma_f32_16x16x32_bf16 v[180:183], v[108:111], v[236:239], v[180:183]\n\t"
        "ds_read_b128 v[232:235], v2 offset:14336\n\t"
        "v_mfma_f32_16x16x32_bf16 v[184:187], v[112:115], v[236:239], v[184:187]\n\t"
        "v_mfma_f32_16x16x32_bf16 v[188:191], v[108:111], v[240:243], v[188:191]\n\t"
        "v_mfma_f32_16x16x32_bf16 v[192:195], v[112:115], v[240:243], v[192:195]\n\t"
        "s_waitcnt lgkmcnt(2)\n\t"
        "v_mfma_f32_16x16x32_bf16 v[196:199], v[108:111], v[244:247], v[196:199]\n\t"
        "v_mfma_f32_16x16x32_bf16 v[200:203], v[112:115], v[244:247], v[200:203]\n\t"
        "v_mfma_f32_16x16x32_bf16 v[204:207], v[108:111], v[248:251], v[204:207]\n\t"
        "v_mfma_f32_16x16x32_bf16 v[208:211], v[112:115], v[248:251], v[208:211]\n\t"
        "s_waitcnt lgkmcnt(0)\n\t"
        "v_mfma_f32_16x16x32_bf16 v[212:215], v[108:111], v[228:231], v[212:215]\n\t"
        "v_mfma_f32_16x16x32_bf16 v[216:219], v[112:115], v[228:231], v[216:219]\n\t"
        "v_mfma_f32_16x16x32_bf16 v[220:223], v[108:111], v[232:235], v[220:223]\n\t"
        "v_mfma_f32_16x16x32_bf16 v[224:227], v[112:115], v[232:235], v[224:227]\n\t"
        "s_cmp_eq_u32 s56, 0\n\t"
        "s_cselect_b32 s70, ((L_cp2_%=-L_begin_%=)&0xffffff80)+4096, s70\n\t"
        "s_cmp_eq_u32 s56, 1\n\t"
        "s_cselect_b32 s70, ((L_cp2_%=-L_begin_%=)&0xffffff80)+5120, s70\n\t"
        "s_cmp_eq_u32 s56, 2\n\t"
        "s_cselect_b32 s70, ((L_cp2_%=-L_begin_%=)&0xffffff80)+6144, s70\n\t"
        "s_cmp_eq_u32 s56, 3\n\t"
        "s_cselect_b32 s70, ((L_cp2_%=-L_begin_%=)&0xffffff80)+7168, s70\n\t"
        "buffer_load_dwordx4 v0, s[28:31], s70 offen lds\n\t"
        "L_cit7_%=:\n\t"
        "s_barrier\n\t"
        "ds_read_b128 v[116:119], v3 offset:16384\n\t"
        "ds_read_b128 v[120:123], v3 offset:17408\n\t"
        "ds_read_b128 v[228:231], v1 offset:20480\n\t"
        "ds_read_b128 v[232:235], v1 offset:30720\n\t"
        "ds_read_b128 v[124:127], v3 offset:24576\n\t"
        "ds_read_b128 v[128:131], v3 offset:25600\n\t"
        "ds_read_b128 v[236:239], v1 offset:21504\n\t"
        "ds_read_b128 v[240:243], v1 offset:31744\n\t"
        "ds_read_b128 v[244:247], v1 offset:22528\n\t"
        "s_waitcnt lgkmcnt(5)\n\t"
        "v_mfma_f32_16x16x32_bf16 v[148:151], v[116:119], v[228:231], v[148:151]\n\t"
        "ds_read_b128 v[248:251], v1 offset:32768\n\t"
        "v_mfma_f32_16x16x32_bf16 v[152:155], v[120:123], v[228:231], v[152:155]\n\t"
        "v_mfma_f32_16x16x32_bf16 v[156:159], v[116:119], v[232:235], v[156:159]\n\t"
        "ds_read_b128 v[228:231], v1 offset:23552\n\t"
        "v_mfma_f32_16x16x32_bf16 v[160:163], v[120:123], v[232:235], v[160:163]\n\t"
        "s_waitcnt lgkmcnt(3)\n\t"
        "v_mfma_f32_16x16x32_bf16 v[164:167], v[116:119], v[236:239], v[164:167]\n\t"
        "ds_read_b128 v[232:235], v1 offset:33792\n\t"
        "v_mfma_f32_16x16x32_bf16 v[168:171], v[120:123], v[236:239], v[168:171]\n\t"
        "v_mfma_f32_16x16x32_bf16 v[172:175], v[116:119], v[240:243], v[172:175]\n\t"
        "ds_read_b128 v[236:239], v1 offset:24576\n\t"
        "v_mfma_f32_16x16x32_bf16 v[176:179], v[120:123], v[240:243], v[176:179]\n\t"
        "s_waitcnt lgkmcnt(3)\n\t"
        "v_mfma_f32_16x16x32_bf16 v[180:183], v[116:119], v[244:247], v[180:183]\n\t"
        "ds_read_b128 v[240:243], v1 offset:34816\n\t"
        "v_mfma_f32_16x16x32_bf16 v[184:187], v[120:123], v[244:247], v[184:187]\n\t"
        "v_mfma_f32_16x16x32_bf16 v[188:191], v[116:119], v[248:251], v[188:191]\n\t"
        "ds_read_b128 v[244:247], v2 offset:20480\n\t"
        "v_mfma_f32_16x16x32_bf16 v[192:195], v[120:123], v[248:251], v[192:195]\n\t"
        "s_waitcnt lgkmcnt(3)\n\t"
        "v_mfma_f32_16x16x32_bf16 v[196:199], v[116:119], v[228:231], v[196:199]\n\t"
        "ds_read_b128 v[248:251], v2 offset:30720\n\t"
        "v_mfma_f32_16x16x32_bf16 v[200:203], v[120:123], v[228:231], v[200:203]\n\t"
        "v_mfma_f32_16x16x32_bf16 v[204:207], v[116:119], v[232:235], v[204:207]\n\t"
        "ds_read_b128 v[228:231], v2 offset:21504\n\t"
        "v_mfma_f32_16x16x32_bf16 v[208:211], v[120:123], v[232:235], v[208:211]\n\t"
        "s_waitcnt lgkmcnt(3)\n\t"
        "v_mfma_f32_16x16x32_bf16 v[212:215], v[116:119], v[236:239], v[212:215]\n\t"
        "ds_read_b128 v[232:235], v2 offset:31744\n\t"
        "v_mfma_f32_16x16x32_bf16 v[216:219], v[120:123], v[236:239], v[216:219]\n\t"
        "v_mfma_f32_16x16x32_bf16 v[220:223], v[116:119], v[240:243], v[220:223]\n\t"
        "ds_read_b128 v[236:239], v2 offset:22528\n\t"
        "v_mfma_f32_16x16x32_bf16 v[224:227], v[120:123], v[240:243], v[224:227]\n\t"
        "s_waitcnt lgkmcnt(3)\n\t"
        "v_mfma_f32_16x16x32_bf16 v[148:151], v[124:127], v[244:247], v[148:151]\n\t"
        "ds_read_b128 v[240:243], v2 offset:32768\n\t"
        "v_mfma_f32_16x16x32_bf16 v[152:155], v[128:131], v[244:247], v[152:155]\n\t"
        "v_mfma_f32_16x16x32_bf16 v[156:159], v[124:127], v[248:251], v[156:159]\n\t"
        "ds_read_b128 v[244:247], v2 offset:23552\n\t"
        "v_mfma_f32_16x16x32_bf16 v[160:163], v[128:131], v[248:251], v[160:163]\n\t"
        "s_waitcnt lgkmcnt(3)\n\t"
        "v_mfma_f32_16x16x32_bf16 v[164:167], v[124:127], v[228:231], v[164:167]\n\t"
        "ds_read_b128 v[248:251], v2 offset:33792\n\t"
        "v_mfma_f32_16x16x32_bf16 v[168:171], v[128:131], v[228:231], v[168:171]\n\t"
        "v_mfma_f32_16x16x32_bf16 v[172:175], v[124:127], v[232:235], v[172:175]\n\t"
        "ds_read_b128 v[228:231], v2 offset:24576\n\t"
        "v_mfma_f32_16x16x32_bf16 v[176:179], v[128:131], v[232:235], v[176:179]\n\t"
        "s_waitcnt lgkmcnt(3)\n\t"
        "v_mfma_f32_16x16x32_bf16 v[180:183], v[124:127], v[236:239], v[180:183]\n\t"
        "ds_read_b128 v[232:235], v2 offset:34816\n\t"
        "v_mfma_f32_16x16x32_bf16 v[184:187], v[128:131], v[236:239], v[184:187]\n\t"
        "v_mfma_f32_16x16x32_bf16 v[188:191], v[124:127], v[240:243], v[188:191]\n\t"
        "v_mfma_f32_16x16x32_bf16 v[192:195], v[128:131], v[240:243], v[192:195]\n\t"
        "s_waitcnt lgkmcnt(2)\n\t"
        "v_mfma_f32_16x16x32_bf16 v[196:199], v[124:127], v[244:247], v[196:199]\n\t"
        "v_mfma_f32_16x16x32_bf16 v[200:203], v[128:131], v[244:247], v[200:203]\n\t"
        "v_mfma_f32_16x16x32_bf16 v[204:207], v[124:127], v[248:251], v[204:207]\n\t"
        "v_mfma_f32_16x16x32_bf16 v[208:211], v[128:131], v[248:251], v[208:211]\n\t"
        "s_waitcnt lgkmcnt(0)\n\t"
        "v_mfma_f32_16x16x32_bf16 v[212:215], v[124:127], v[228:231], v[212:215]\n\t"
        "v_mfma_f32_16x16x32_bf16 v[216:219], v[128:131], v[228:231], v[216:219]\n\t"
        "v_mfma_f32_16x16x32_bf16 v[220:223], v[124:127], v[232:235], v[220:223]\n\t"
        "v_mfma_f32_16x16x32_bf16 v[224:227], v[128:131], v[232:235], v[224:227]\n\t"
        "s_cmp_eq_u32 s56, 0\n\t"
        "s_cselect_b32 s70, ((L_cp2_%=-L_begin_%=)&0xffffff80)+8192, s70\n\t"
        "s_cmp_eq_u32 s56, 1\n\t"
        "s_cselect_b32 s70, ((L_cp2_%=-L_begin_%=)&0xffffff80)+9216, s70\n\t"
        "s_cmp_eq_u32 s56, 2\n\t"
        "s_cselect_b32 s70, ((L_st_%=-L_begin_%=)&0xffffff80)+2048, s70\n\t"
        "s_cmp_eq_u32 s56, 3\n\t"
        "s_cselect_b32 s70, ((L_ppost_%=-L_begin_%=)&0xffffff80)+2048, s70\n\t"
        "buffer_load_dwordx4 v0, s[28:31], s70 offen lds\n\t"
        "L_cit8_%=:\n\t"
        "s_barrier\n\t"
        "ds_read_b128 v[132:135], v3 offset:0\n\t"
        "ds_read_b128 v[136:139], v3 offset:1024\n\t"
        "ds_read_b128 v[228:231], v1 offset:40960\n\t"
        "ds_read_b128 v[232:235], v1 offset:51200\n\t"
        "ds_read_b128 v[140:143], v3 offset:8192\n\t"
        "ds_read_b128 v[144:147], v3 offset:9216\n\t"
        "ds_read_b128 v[236:239], v1 offset:41984\n\t"
        "ds_read_b128 v[240:243], v1 offset:52224\n\t"
        "ds_read_b128 v[244:247], v1 offset:43008\n\t"
        "s_waitcnt lgkmcnt(5)\n\t"
        "v_mfma_f32_16x16x32_bf16 v[148:151], v[132:135], v[228:231], v[148:151]\n\t"
        "ds_read_b128 v[248:251], v1 offset:53248\n\t"
        "v_mfma_f32_16x16x32_bf16 v[152:155], v[136:139], v[228:231], v[152:155]\n\t"
        "v_mfma_f32_16x16x32_bf16 v[156:159], v[132:135], v[232:235], v[156:159]\n\t"
        "ds_read_b128 v[228:231], v1 offset:44032\n\t"
        "v_mfma_f32_16x16x32_bf16 v[160:163], v[136:139], v[232:235], v[160:163]\n\t"
        "s_waitcnt lgkmcnt(3)\n\t"
        "v_mfma_f32_16x16x32_bf16 v[164:167], v[132:135], v[236:239], v[164:167]\n\t"
        "ds_read_b128 v[232:235], v1 offset:54272\n\t"
        "v_mfma_f32_16x16x32_bf16 v[168:171], v[136:139], v[236:239], v[168:171]\n\t"
        "v_mfma_f32_16x16x32_bf16 v[172:175], v[132:135], v[240:243], v[172:175]\n\t"
        "ds_read_b128 v[236:239], v1 offset:45056\n\t"
        "v_mfma_f32_16x16x32_bf16 v[176:179], v[136:139], v[240:243], v[176:179]\n\t"
        "s_waitcnt lgkmcnt(3)\n\t"
        "v_mfma_f32_16x16x32_bf16 v[180:183], v[132:135], v[244:247], v[180:183]\n\t"
        "ds_read_b128 v[240:243], v1 offset:55296\n\t"
        "v_mfma_f32_16x16x32_bf16 v[184:187], v[136:139], v[244:247], v[184:187]\n\t"
        "v_mfma_f32_16x16x32_bf16 v[188:191], v[132:135], v[248:251], v[188:191]\n\t"
        "ds_read_b128 v[244:247], v2 offset:40960\n\t"
        "v_mfma_f32_16x16x32_bf16 v[192:195], v[136:139], v[248:251], v[192:195]\n\t"
        "s_waitcnt lgkmcnt(3)\n\t"
        "v_mfma_f32_16x16x32_bf16 v[196:199], v[132:135], v[228:231], v[196:199]\n\t"
        "ds_read_b128 v[248:251], v2 offset:51200\n\t"
        "v_mfma_f32_16x16x32_bf16 v[200:203], v[136:139], v[228:231], v[200:203]\n\t"
        "v_mfma_f32_16x16x32_bf16 v[204:207], v[132:135], v[232:235], v[204:207]\n\t"
        "ds_read_b128 v[228:231], v2 offset:41984\n\t"
        "v_mfma_f32_16x16x32_bf16 v[208:211], v[136:139], v[232:235], v[208:211]\n\t"
        "s_waitcnt lgkmcnt(3)\n\t"
        "v_mfma_f32_16x16x32_bf16 v[212:215], v[132:135], v[236:239], v[212:215]\n\t"
        "ds_read_b128 v[232:235], v2 offset:52224\n\t"
        "v_mfma_f32_16x16x32_bf16 v[216:219], v[136:139], v[236:239], v[216:219]\n\t"
        "v_mfma_f32_16x16x32_bf16 v[220:223], v[132:135], v[240:243], v[220:223]\n\t"
        "ds_read_b128 v[236:239], v2 offset:43008\n\t"
        "v_mfma_f32_16x16x32_bf16 v[224:227], v[136:139], v[240:243], v[224:227]\n\t"
        "s_waitcnt lgkmcnt(3)\n\t"
        "v_mfma_f32_16x16x32_bf16 v[148:151], v[140:143], v[244:247], v[148:151]\n\t"
        "ds_read_b128 v[240:243], v2 offset:53248\n\t"
        "v_mfma_f32_16x16x32_bf16 v[152:155], v[144:147], v[244:247], v[152:155]\n\t"
        "v_mfma_f32_16x16x32_bf16 v[156:159], v[140:143], v[248:251], v[156:159]\n\t"
        "ds_read_b128 v[244:247], v2 offset:44032\n\t"
        "v_mfma_f32_16x16x32_bf16 v[160:163], v[144:147], v[248:251], v[160:163]\n\t"
        "s_waitcnt lgkmcnt(3)\n\t"
        "v_mfma_f32_16x16x32_bf16 v[164:167], v[140:143], v[228:231], v[164:167]\n\t"
        "ds_read_b128 v[248:251], v2 offset:54272\n\t"
        "v_mfma_f32_16x16x32_bf16 v[168:171], v[144:147], v[228:231], v[168:171]\n\t"
        "v_mfma_f32_16x16x32_bf16 v[172:175], v[140:143], v[232:235], v[172:175]\n\t"
        "ds_read_b128 v[228:231], v2 offset:45056\n\t"
        "v_mfma_f32_16x16x32_bf16 v[176:179], v[144:147], v[232:235], v[176:179]\n\t"
        "s_waitcnt lgkmcnt(3)\n\t"
        "v_mfma_f32_16x16x32_bf16 v[180:183], v[140:143], v[236:239], v[180:183]\n\t"
        "ds_read_b128 v[232:235], v2 offset:55296\n\t"
        "v_mfma_f32_16x16x32_bf16 v[184:187], v[144:147], v[236:239], v[184:187]\n\t"
        "v_mfma_f32_16x16x32_bf16 v[188:191], v[140:143], v[240:243], v[188:191]\n\t"
        "v_mfma_f32_16x16x32_bf16 v[192:195], v[144:147], v[240:243], v[192:195]\n\t"
        "s_waitcnt lgkmcnt(2)\n\t"
        "v_mfma_f32_16x16x32_bf16 v[196:199], v[140:143], v[244:247], v[196:199]\n\t"
        "v_mfma_f32_16x16x32_bf16 v[200:203], v[144:147], v[244:247], v[200:203]\n\t"
        "v_mfma_f32_16x16x32_bf16 v[204:207], v[140:143], v[248:251], v[204:207]\n\t"
        "v_mfma_f32_16x16x32_bf16 v[208:211], v[144:147], v[248:251], v[208:211]\n\t"
        "s_waitcnt lgkmcnt(0)\n\t"
        "v_mfma_f32_16x16x32_bf16 v[212:215], v[140:143], v[228:231], v[212:215]\n\t"
        "v_mfma_f32_16x16x32_bf16 v[216:219], v[144:147], v[228:231], v[216:219]\n\t"
        "v_mfma_f32_16x16x32_bf16 v[220:223], v[140:143], v[232:235], v[220:223]\n\t"
        "v_mfma_f32_16x16x32_bf16 v[224:227], v[144:147], v[232:235], v[224:227]\n\t"
        "L_cp2_%=:\n\t"
        "s_nop 7\n\t"
        "v_cvt_pk_bf16_f32 v148, v148, v152\n\t"
        "v_cvt_pk_bf16_f32 v149, v149, v153\n\t"
        "v_cvt_pk_bf16_f32 v150, v150, v154\n\t"
        "v_cvt_pk_bf16_f32 v151, v151, v155\n\t"
        "v_cvt_pk_bf16_f32 v156, v156, v160\n\t"
        "v_cvt_pk_bf16_f32 v157, v157, v161\n\t"
        "v_cvt_pk_bf16_f32 v158, v158, v162\n\t"
        "v_cvt_pk_bf16_f32 v159, v159, v163\n\t"
        "v_add_u32 v1, 126976, v255\n\t"
        "v_xor_b32 v2, 64, v1\n\t"
        "s_waitcnt vmcnt(0)\n\t"
        "s_barrier\n\t"
        "ds_read_b128 v[228:231], v1 offset:0\n\t"
        "ds_read_b128 v[232:235], v2 offset:0\n\t"
        "ds_read_b128 v[236:239], v1 offset:1024\n\t"
        "ds_read_b128 v[240:243], v2 offset:1024\n\t"
        "ds_read_b128 v[244:247], v1 offset:2048\n\t"
        "s_waitcnt lgkmcnt(3)\n\t"
        "v_mfma_f32_16x16x32_bf16 v[152:155], v[4:7], v[228:231], 0\n\t"
        "ds_read_b128 v[248:251], v2 offset:2048\n\t"
        "v_mfma_f32_16x16x32_bf16 v[160:163], v[8:11], v[228:231], 0\n\t"
        "ds_write_b64 v253, v[148:149] offset:0\n\t"
        "v_mfma_f32_16x16x32_bf16 v[152:155], v[12:15], v[232:235], v[152:155]\n\t"
        "ds_read_b128 v[228:231], v1 offset:3072\n\t"
        "v_mfma_f32_16x16x32_bf16 v[160:163], v[16:19], v[232:235], v[160:163]\n\t"
        "ds_write_b64 v254, v[150:151] offset:0\n\t"
        "s_waitcnt lgkmcnt(5)\n\t"
        "v_mfma_f32_16x16x32_bf16 v[152:155], v[20:23], v[236:239], v[152:155]\n\t"
        "ds_read_b128 v[232:235], v2 offset:3072\n\t"
        "v_mfma_f32_16x16x32_bf16 v[160:163], v[24:27], v[236:239], v[160:163]\n\t"
        "v_mfma_f32_16x16x32_bf16 v[152:155], v[28:31], v[240:243], v[152:155]\n\t"
        "ds_read_b128 v[236:239], v1 offset:4096\n\t"
        "v_mfma_f32_16x16x32_bf16 v[160:163], v[32:35], v[240:243], v[160:163]\n\t"
        "ds_write_b64 v253, v[156:157] offset:4096\n\t"
        "s_waitcnt lgkmcnt(6)\n\t"
        "v_mfma_f32_16x16x32_bf16 v[152:155], v[36:39], v[244:247], v[152:155]\n\t"
        "ds_read_b128 v[240:243], v2 offset:4096\n\t"
        "v_mfma_f32_16x16x32_bf16 v[160:163], v[40:43], v[244:247], v[160:163]\n\t"
        "ds_write_b64 v254, v[158:159] offset:4096\n\t"
        "v_mfma_f32_16x16x32_bf16 v[152:155], v[44:47], v[248:251], v[152:155]\n\t"
        "ds_read_b128 v[244:247], v1 offset:5120\n\t"
        "v_mfma_f32_16x16x32_bf16 v[160:163], v[48:51], v[248:251], v[160:163]\n\t"
        "s_waitcnt lgkmcnt(5)\n\t"
        "v_mfma_f32_16x16x32_bf16 v[152:155], v[52:55], v[228:231], v[152:155]\n\t"
        "ds_read_b128 v[248:251], v2 offset:5120\n\t"
        "v_mfma_f32_16x16x32_bf16 v[160:163], v[56:59], v[228:231], v[160:163]\n\t"
        "v_mfma_f32_16x16x32_bf16 v[152:155], v[60:63], v[232:235], v[152:155]\n\t"
        "ds_read_b128 v[228:231], v1 offset:6144\n\t"
        "v_mfma_f32_16x16x32_bf16 v[160:163], v[64:67], v[232:235], v[160:163]\n\t"
        "s_waitcnt lgkmcnt(4)\n\t"
        "v_mfma_f32_16x16x32_bf16 v[152:155], v[68:71], v[236:239], v[152:155]\n\t"
        "ds_read_b128 v[232:235], v2 offset:6144\n\t"
        "v_mfma_f32_16x16x32_bf16 v[160:163], v[72:75], v[236:239], v[160:163]\n\t"
        "v_mfma_f32_16x16x32_bf16 v[152:155], v[76:79], v[240:243], v[152:155]\n\t"
        "ds_read_b128 v[236:239], v1 offset:7168\n\t"
        "v_mfma_f32_16x16x32_bf16 v[160:163], v[80:83], v[240:243], v[160:163]\n\t"
        "s_waitcnt lgkmcnt(3)\n\t"
        "v_mfma_f32_16x16x32_bf16 v[152:155], v[84:87], v[244:247], v[152:155]\n\t"
        "ds_read_b128 v[240:243], v2 offset:7168\n\t"
        "v_mfma_f32_16x16x32_bf16 v[160:163], v[88:91], v[244:247], v[160:163]\n\t"
        "v_mfma_f32_16x16x32_bf16 v[152:155], v[92:95], v[248:251], v[152:155]\n\t"
        "ds_read_b128 v[244:247], v1 offset:8192\n\t"
        "v_mfma_f32_16x16x32_bf16 v[160:163], v[96:99], v[248:251], v[160:163]\n\t"
        "s_barrier\n\t"
        "s_waitcnt lgkmcnt(3)\n\t"
        "v_mfma_f32_16x16x32_bf16 v[152:155], v[100:103], v[228:231], v[152:155]\n\t"
        "ds_read_b128 v[248:251], v2 offset:8192\n\t"
        "v_mfma_f32_16x16x32_bf16 v[160:163], v[104:107], v[228:231], v[160:163]\n\t"
        "v_cvt_pk_bf16_f32 v164, v164, v168\n\t"
        "v_mfma_f32_16x16x32_bf16 v[152:155], v[108:111], v[232:235], v[152:155]\n\t"
        "v_add_u32 v1, 18432, v1\n\t"
        "v_add_u32 v2, 18432, v2\n\t"
        "ds_read_b128 v[228:231], v1 offset:0\n\t"
        "v_mfma_f32_16x16x32_bf16 v[160:163], v[112:115], v[232:235], v[160:163]\n\t"
        "v_cvt_pk_bf16_f32 v165, v165, v169\n\t"
        "s_waitcnt lgkmcnt(3)\n\t"
        "v_mfma_f32_16x16x32_bf16 v[152:155], v[116:119], v[236:239], v[152:155]\n\t"
        "ds_read_b128 v[232:235], v2 offset:0\n\t"
        "v_mfma_f32_16x16x32_bf16 v[160:163], v[120:123], v[236:239], v[160:163]\n\t"
        "v_cvt_pk_bf16_f32 v166, v166, v170\n\t"
        "v_mfma_f32_16x16x32_bf16 v[152:155], v[124:127], v[240:243], v[152:155]\n\t"
        "ds_read_b128 v[236:239], v1 offset:1024\n\t"
        "v_mfma_f32_16x16x32_bf16 v[160:163], v[128:131], v[240:243], v[160:163]\n\t"
        "v_cvt_pk_bf16_f32 v167, v167, v171\n\t"
        "s_waitcnt lgkmcnt(3)\n\t"
        "v_mfma_f32_16x16x32_bf16 v[152:155], v[132:135], v[244:247], v[152:155]\n\t"
        "ds_read_b128 v[240:243], v2 offset:1024\n\t"
        "v_mfma_f32_16x16x32_bf16 v[160:163], v[136:139], v[244:247], v[160:163]\n\t"
        "ds_write_b64 v253, v[164:165] offset:8192\n\t"
        "v_mfma_f32_16x16x32_bf16 v[152:155], v[140:143], v[248:251], v[152:155]\n\t"
        "ds_read_b128 v[244:247], v1 offset:2048\n\t"
        "v_mfma_f32_16x16x32_bf16 v[160:163], v[144:147], v[248:251], v[160:163]\n\t"
        "ds_write_b64 v254, v[166:167] offset:8192\n\t"
        "s_waitcnt lgkmcnt(5)\n\t"
        "v_mfma_f32_16x16x32_bf16 v[148:151], v[4:7], v[228:231], 0\n\t"
        "ds_read_b128 v[248:251], v2 offset:2048\n\t"
        "v_mfma_f32_16x16x32_bf16 v[156:159], v[8:11], v[228:231], 0\n\t"
        "v_mfma_f32_16x16x32_bf16 v[148:151], v[12:15], v[232:235], v[148:151]\n\t"
        "ds_read_b128 v[228:231], v1 offset:3072\n\t"
        "v_mfma_f32_16x16x32_bf16 v[156:159], v[16:19], v[232:235], v[156:159]\n\t"
        "s_waitcnt lgkmcnt(5)\n\t"
        "v_mfma_f32_16x16x32_bf16 v[148:151], v[20:23], v[236:239], v[148:151]\n\t"
        "ds_read_b128 v[232:235], v2 offset:3072\n\t"
        "v_mfma_f32_16x16x32_bf16 v[156:159], v[24:27], v[236:239], v[156:159]\n\t"
        "v_cvt_pk_bf16_f32 v152, v152, v160\n\t"
        "v_mfma_f32_16x16x32_bf16 v[148:151], v[28:31], v[240:243], v[148:151]\n\t"
        "ds_read_b128 v[236:239], v1 offset:4096\n\t"
        "v_mfma_f32_16x16x32_bf16 v[156:159], v[32:35], v[240:243], v[156:159]\n\t"
        "v_cvt_pk_bf16_f32 v153, v153, v161\n\t"
        "s_waitcnt lgkmcnt(3)\n\t"
        "v_mfma_f32_16x16x32_bf16 v[148:151], v[36:39], v[244:247], v[148:151]\n\t"
        "ds_read_b128 v[240:243], v2 offset:4096\n\t"
        "v_mfma_f32_16x16x32_bf16 v[156:159], v[40:43], v[244:247], v[156:159]\n\t"
        "v_cvt_pk_bf16_f32 v154, v154, v162\n\t"
        "v_mfma_f32_16x16x32_bf16 v[148:151], v[44:47], v[248:251], v[148:151]\n\t"
        "ds_read_b128 v[244:247], v1 offset:5120\n\t"
        "v_mfma_f32_16x16x32_bf16 v[156:159], v[48:51], v[248:251], v[156:159]\n\t"
        "v_cvt_pk_bf16_f32 v155, v155, v163\n\t"
        "s_waitcnt lgkmcnt(3)\n\t"
        "v_mfma_f32_16x16x32_bf16 v[148:151], v[52:55], v[228:231], v[148:151]\n\t"
        "ds_read_b128 v[248:251], v2 offset:5120\n\t"
        "v_mfma_f32_16x16x32_bf16 v[156:159], v[56:59], v[228:231], v[156:159]\n\t"
        "ds_write_b64 v253, v[152:153] offset:12288\n\t"
        "v_mfma_f32_16x16x32_bf16 v[148:151], v[60:63], v[232:235], v[148:151]\n\t"
        "ds_read_b128 v[228:231], v1 offset:6144\n\t"
        "v_mfma_f32_16x16x32_bf16 v[156:159], v[64:67], v[232:235], v[156:159]\n\t"
        "ds_write_b64 v254, v[154:155] offset:12288\n\t"
        "s_waitcnt lgkmcnt(5)\n\t"
        "v_mfma_f32_16x16x32_bf16 v[148:151], v[68:71], v[236:239], v[148:151]\n\t"
        "ds_read_b128 v[232:235], v2 offset:6144\n\t"
        "v_mfma_f32_16x16x32_bf16 v[156:159], v[72:75], v[236:239], v[156:159]\n\t"
        "v_mfma_f32_16x16x32_bf16 v[148:151], v[76:79], v[240:243], v[148:151]\n\t"
        "ds_read_b128 v[236:239], v1 offset:7168\n\t"
        "v_mfma_f32_16x16x32_bf16 v[156:159], v[80:83], v[240:243], v[156:159]\n\t"
        "s_waitcnt lgkmcnt(5)\n\t"
        "v_mfma_f32_16x16x32_bf16 v[148:151], v[84:87], v[244:247], v[148:151]\n\t"
        "ds_read_b128 v[240:243], v2 offset:7168\n\t"
        "v_mfma_f32_16x16x32_bf16 v[156:159], v[88:91], v[244:247], v[156:159]\n\t"
        "v_mfma_f32_16x16x32_bf16 v[148:151], v[92:95], v[248:251], v[148:151]\n\t"
        "ds_read_b128 v[244:247], v1 offset:8192\n\t"
        "v_mfma_f32_16x16x32_bf16 v[156:159], v[96:99], v[248:251], v[156:159]\n\t"
        "s_waitcnt lgkmcnt(4)\n\t"
        "s_barrier\n\t"
        "s_waitcnt lgkmcnt(3)\n\t"
        "v_mfma_f32_16x16x32_bf16 v[148:151], v[100:103], v[228:231], v[148:151]\n\t"
        "ds_read_b128 v[248:251], v2 offset:8192\n\t"
        "v_mfma_f32_16x16x32_bf16 v[156:159], v[104:107], v[228:231], v[156:159]\n\t"
        "v_cvt_pk_bf16_f32 v172, v172, v176\n\t"
        "v_mfma_f32_16x16x32_bf16 v[148:151], v[108:111], v[232:235], v[148:151]\n\t"
        "v_subrev_u32 v1, 145408, v1\n\t"
        "v_subrev_u32 v2, 145408, v2\n\t"
        "ds_read_b128 v[228:231], v1 offset:0\n\t"
        "v_mfma_f32_16x16x32_bf16 v[156:159], v[112:115], v[232:235], v[156:159]\n\t"
        "v_cvt_pk_bf16_f32 v173, v173, v177\n\t"
        "s_waitcnt lgkmcnt(3)\n\t"
        "v_mfma_f32_16x16x32_bf16 v[148:151], v[116:119], v[236:239], v[148:151]\n\t"
        "ds_read_b128 v[232:235], v2 offset:0\n\t"
        "v_mfma_f32_16x16x32_bf16 v[156:159], v[120:123], v[236:239], v[156:159]\n\t"
        "v_cvt_pk_bf16_f32 v174, v174, v178\n\t"
        "v_mfma_f32_16x16x32_bf16 v[148:151], v[124:127], v[240:243], v[148:151]\n\t"
        "ds_read_b128 v[236:239], v1 offset:1024\n\t"
        "v_mfma_f32_16x16x32_bf16 v[156:159], v[128:131], v[240:243], v[156:159]\n\t"
        "v_cvt_pk_bf16_f32 v175, v175, v179\n\t"
        "s_waitcnt lgkmcnt(3)\n\t"
        "v_mfma_f32_16x16x32_bf16 v[148:151], v[132:135], v[244:247], v[148:151]\n\t"
        "ds_read_b128 v[240:243], v2 offset:1024\n\t"
        "v_mfma_f32_16x16x32_bf16 v[156:159], v[136:139], v[244:247], v[156:159]\n\t"
        "ds_write_b64 v253, v[172:173] offset:0\n\t"
        "v_mfma_f32_16x16x32_bf16 v[148:151], v[140:143], v[248:251], v[148:151]\n\t"
        "ds_read_b128 v[244:247], v1 offset:2048\n\t"
        "v_mfma_f32_16x16x32_bf16 v[156:159], v[144:147], v[248:251], v[156:159]\n\t"
        "ds_write_b64 v254, v[174:175] offset:0\n\t"
        "s_waitcnt lgkmcnt(5)\n\t"
        "v_mfma_f32_16x16x32_bf16 v[168:171], v[4:7], v[228:231], 0\n\t"
        "ds_read_b128 v[248:251], v2 offset:2048\n\t"
        "v_mfma_f32_16x16x32_bf16 v[164:167], v[8:11], v[228:231], 0\n\t"
        "v_mfma_f32_16x16x32_bf16 v[168:171], v[12:15], v[232:235], v[168:171]\n\t"
        "ds_read_b128 v[228:231], v1 offset:3072\n\t"
        "v_mfma_f32_16x16x32_bf16 v[164:167], v[16:19], v[232:235], v[164:167]\n\t"
        "s_waitcnt lgkmcnt(5)\n\t"
        "v_mfma_f32_16x16x32_bf16 v[168:171], v[20:23], v[236:239], v[168:171]\n\t"
        "ds_read_b128 v[232:235], v2 offset:3072\n\t"
        "v_mfma_f32_16x16x32_bf16 v[164:167], v[24:27], v[236:239], v[164:167]\n\t"
        "v_cvt_pk_bf16_f32 v148, v148, v156\n\t"
        "v_mfma_f32_16x16x32_bf16 v[168:171], v[28:31], v[240:243], v[168:171]\n\t"
        "ds_read_b128 v[236:239], v1 offset:4096\n\t"
        "v_mfma_f32_16x16x32_bf16 v[164:167], v[32:35], v[240:243], v[164:167]\n\t"
        "v_cvt_pk_bf16_f32 v149, v149, v157\n\t"
        "s_waitcnt lgkmcnt(3)\n\t"
        "v_mfma_f32_16x16x32_bf16 v[168:171], v[36:39], v[244:247], v[168:171]\n\t"
        "ds_read_b128 v[240:243], v2 offset:4096\n\t"
        "v_mfma_f32_16x16x32_bf16 v[164:167], v[40:43], v[244:247], v[164:167]\n\t"
        "v_cvt_pk_bf16_f32 v150, v150, v158\n\t"
        "v_mfma_f32_16x16x32_bf16 v[168:171], v[44:47], v[248:251], v[168:171]\n\t"
        "ds_read_b128 v[244:247], v1 offset:5120\n\t"
        "v_mfma_f32_16x16x32_bf16 v[164:167], v[48:51], v[248:251], v[164:167]\n\t"
        "v_cvt_pk_bf16_f32 v151, v151, v159\n\t"
        "s_waitcnt lgkmcnt(3)\n\t"
        "v_mfma_f32_16x16x32_bf16 v[168:171], v[52:55], v[228:231], v[168:171]\n\t"
        "ds_read_b128 v[248:251], v2 offset:5120\n\t"
        "v_mfma_f32_16x16x32_bf16 v[164:167], v[56:59], v[228:231], v[164:167]\n\t"
        "ds_write_b64 v253, v[148:149] offset:4096\n\t"
        "v_mfma_f32_16x16x32_bf16 v[168:171], v[60:63], v[232:235], v[168:171]\n\t"
        "ds_read_b128 v[228:231], v1 offset:6144\n\t"
        "v_mfma_f32_16x16x32_bf16 v[164:167], v[64:67], v[232:235], v[164:167]\n\t"
        "ds_write_b64 v254, v[150:151] offset:4096\n\t"
        "s_waitcnt lgkmcnt(5)\n\t"
        "v_mfma_f32_16x16x32_bf16 v[168:171], v[68:71], v[236:239], v[168:171]\n\t"
        "ds_read_b128 v[232:235], v2 offset:6144\n\t"
        "v_mfma_f32_16x16x32_bf16 v[164:167], v[72:75], v[236:239], v[164:167]\n\t"
        "v_mfma_f32_16x16x32_bf16 v[168:171], v[76:79], v[240:243], v[168:171]\n\t"
        "ds_read_b128 v[236:239], v1 offset:7168\n\t"
        "v_mfma_f32_16x16x32_bf16 v[164:167], v[80:83], v[240:243], v[164:167]\n\t"
        "s_waitcnt lgkmcnt(5)\n\t"
        "v_mfma_f32_16x16x32_bf16 v[168:171], v[84:87], v[244:247], v[168:171]\n\t"
        "ds_read_b128 v[240:243], v2 offset:7168\n\t"
        "v_mfma_f32_16x16x32_bf16 v[164:167], v[88:91], v[244:247], v[164:167]\n\t"
        "v_mfma_f32_16x16x32_bf16 v[168:171], v[92:95], v[248:251], v[168:171]\n\t"
        "ds_read_b128 v[244:247], v1 offset:8192\n\t"
        "v_mfma_f32_16x16x32_bf16 v[164:167], v[96:99], v[248:251], v[164:167]\n\t"
        "s_waitcnt lgkmcnt(4)\n\t"
        "s_barrier\n\t"
        "s_waitcnt lgkmcnt(3)\n\t"
        "v_mfma_f32_16x16x32_bf16 v[168:171], v[100:103], v[228:231], v[168:171]\n\t"
        "ds_read_b128 v[248:251], v2 offset:8192\n\t"
        "v_mfma_f32_16x16x32_bf16 v[164:167], v[104:107], v[228:231], v[164:167]\n\t"
        "v_cvt_pk_bf16_f32 v180, v180, v184\n\t"
        "v_mfma_f32_16x16x32_bf16 v[168:171], v[108:111], v[232:235], v[168:171]\n\t"
        "v_add_u32 v1, 18432, v1\n\t"
        "v_add_u32 v2, 18432, v2\n\t"
        "ds_read_b128 v[228:231], v1 offset:0\n\t"
        "v_mfma_f32_16x16x32_bf16 v[164:167], v[112:115], v[232:235], v[164:167]\n\t"
        "v_cvt_pk_bf16_f32 v181, v181, v185\n\t"
        "s_waitcnt lgkmcnt(3)\n\t"
        "v_mfma_f32_16x16x32_bf16 v[168:171], v[116:119], v[236:239], v[168:171]\n\t"
        "ds_read_b128 v[232:235], v2 offset:0\n\t"
        "v_mfma_f32_16x16x32_bf16 v[164:167], v[120:123], v[236:239], v[164:167]\n\t"
        "v_cvt_pk_bf16_f32 v182, v182, v186\n\t"
        "v_mfma_f32_16x16x32_bf16 v[168:171], v[124:127], v[240:243], v[168:171]\n\t"
        "ds_read_b128 v[236:239], v1 offset:1024\n\t"
        "v_mfma_f32_16x16x32_bf16 v[164:167], v[128:131], v[240:243], v[164:167]\n\t"
        "v_cvt_pk_bf16_f32 v183, v183, v187\n\t"
        "s_waitcnt lgkmcnt(3)\n\t"
        "v_mfma_f32_16x16x32_bf16 v[168:171], v[132:135], v[244:247], v[168:171]\n\t"
        "ds_read_b128 v[240:243], v2 offset:1024\n\t"
        "v_mfma_f32_16x16x32_bf16 v[164:167], v[136:139], v[244:247], v[164:167]\n\t"
        "ds_write_b64 v253, v[180:181] offset:8192\n\t"
        "v_mfma_f32_16x16x32_bf16 v[168:171], v[140:143], v[248:251], v[168:171]\n\t"
        "ds_read_b128 v[244:247], v1 offset:2048\n\t"
        "v_mfma_f32_16x16x32_bf16 v[164:167], v[144:147], v[248:251], v[164:167]\n\t"
        "ds_write_b64 v254, v[182:183] offset:8192\n\t"
        "s_waitcnt lgkmcnt(5)\n\t"
        "v_mfma_f32_16x16x32_bf16 v[160:163], v[4:7], v[228:231], 0\n\t"
        "ds_read_b128 v[248:251], v2 offset:2048\n\t"
        "v_mfma_f32_16x16x32_bf16 v[152:155], v[8:11], v[228:231], 0\n\t"
        "v_mfma_f32_16x16x32_bf16 v[160:163], v[12:15], v[232:235], v[160:163]\n\t"
        "ds_read_b128 v[228:231], v1 offset:3072\n\t"
        "v_mfma_f32_16x16x32_bf16 v[152:155], v[16:19], v[232:235], v[152:155]\n\t"
        "s_waitcnt lgkmcnt(5)\n\t"
        "v_mfma_f32_16x16x32_bf16 v[160:163], v[20:23], v[236:239], v[160:163]\n\t"
        "ds_read_b128 v[232:235], v2 offset:3072\n\t"
        "v_mfma_f32_16x16x32_bf16 v[152:155], v[24:27], v[236:239], v[152:155]\n\t"
        "v_cvt_pk_bf16_f32 v168, v168, v164\n\t"
        "v_mfma_f32_16x16x32_bf16 v[160:163], v[28:31], v[240:243], v[160:163]\n\t"
        "ds_read_b128 v[236:239], v1 offset:4096\n\t"
        "v_mfma_f32_16x16x32_bf16 v[152:155], v[32:35], v[240:243], v[152:155]\n\t"
        "v_cvt_pk_bf16_f32 v169, v169, v165\n\t"
        "s_waitcnt lgkmcnt(3)\n\t"
        "v_mfma_f32_16x16x32_bf16 v[160:163], v[36:39], v[244:247], v[160:163]\n\t"
        "ds_read_b128 v[240:243], v2 offset:4096\n\t"
        "v_mfma_f32_16x16x32_bf16 v[152:155], v[40:43], v[244:247], v[152:155]\n\t"
        "v_cvt_pk_bf16_f32 v170, v170, v166\n\t"
        "v_mfma_f32_16x16x32_bf16 v[160:163], v[44:47], v[248:251], v[160:163]\n\t"
        "ds_read_b128 v[244:247], v1 offset:5120\n\t"
        "v_mfma_f32_16x16x32_bf16 v[152:155], v[48:51], v[248:251], v[152:155]\n\t"
        "v_cvt_pk_bf16_f32 v171, v171, v167\n\t"
        "s_waitcnt lgkmcnt(3)\n\t"
        "v_mfma_f32_16x16x32_bf16 v[160:163], v[52:55], v[228:231], v[160:163]\n\t"
        "ds_read_b128 v[248:251], v2 offset:5120\n\t"
        "v_mfma_f32_16x16x32_bf16 v[152:155], v[56:59], v[228:231], v[152:155]\n\t"
        "ds_write_b64 v253, v[168:169] offset:12288\n\t"
        "v_mfma_f32_16x16x32_bf16 v[160:163], v[60:63], v[232:235], v[160:163]\n\t"
        "ds_read_b128 v[228:231], v1 offset:6144\n\t"
        "v_mfma_f32_16x16x32_bf16 v[152:155], v[64:67], v[232:235], v[152:155]\n\t"
        "ds_write_b64 v254, v[170:171] offset:12288\n\t"
        "s_waitcnt lgkmcnt(5)\n\t"
        "v_mfma_f32_16x16x32_bf16 v[160:163], v[68:71], v[236:239], v[160:163]\n\t"
        "ds_read_b128 v[232:235], v2 offset:6144\n\t"
        "v_mfma_f32_16x16x32_bf16 v[152:155], v[72:75], v[236:239], v[152:155]\n\t"
        "v_mfma_f32_16x16x32_bf16 v[160:163], v[76:79], v[240:243], v[160:163]\n\t"
        "ds_read_b128 v[236:239], v1 offset:7168\n\t"
        "v_mfma_f32_16x16x32_bf16 v[152:155], v[80:83], v[240:243], v[152:155]\n\t"
        "s_waitcnt lgkmcnt(5)\n\t"
        "v_mfma_f32_16x16x32_bf16 v[160:163], v[84:87], v[244:247], v[160:163]\n\t"
        "ds_read_b128 v[240:243], v2 offset:7168\n\t"
        "v_mfma_f32_16x16x32_bf16 v[152:155], v[88:91], v[244:247], v[152:155]\n\t"
        "v_mfma_f32_16x16x32_bf16 v[160:163], v[92:95], v[248:251], v[160:163]\n\t"
        "ds_read_b128 v[244:247], v1 offset:8192\n\t"
        "v_mfma_f32_16x16x32_bf16 v[152:155], v[96:99], v[248:251], v[152:155]\n\t"
        "s_waitcnt lgkmcnt(4)\n\t"
        "s_barrier\n\t"
        "s_waitcnt lgkmcnt(3)\n\t"
        "v_mfma_f32_16x16x32_bf16 v[160:163], v[100:103], v[228:231], v[160:163]\n\t"
        "ds_read_b128 v[248:251], v2 offset:8192\n\t"
        "v_mfma_f32_16x16x32_bf16 v[152:155], v[104:107], v[228:231], v[152:155]\n\t"
        "v_cvt_pk_bf16_f32 v188, v188, v192\n\t"
        "v_mfma_f32_16x16x32_bf16 v[160:163], v[108:111], v[232:235], v[160:163]\n\t"
        "v_add_u32 v1, 18432, v1\n\t"
        "v_add_u32 v2, 18432, v2\n\t"
        "ds_read_b128 v[228:231], v1 offset:0\n\t"
        "v_mfma_f32_16x16x32_bf16 v[152:155], v[112:115], v[232:235], v[152:155]\n\t"
        "v_cvt_pk_bf16_f32 v189, v189, v193\n\t"
        "s_waitcnt lgkmcnt(3)\n\t"
        "v_mfma_f32_16x16x32_bf16 v[160:163], v[116:119], v[236:239], v[160:163]\n\t"
        "ds_read_b128 v[232:235], v2 offset:0\n\t"
        "v_mfma_f32_16x16x32_bf16 v[152:155], v[120:123], v[236:239], v[152:155]\n\t"
        "v_cvt_pk_bf16_f32 v190, v190, v194\n\t"
        "v_mfma_f32_16x16x32_bf16 v[160:163], v[124:127], v[240:243], v[160:163]\n\t"
        "ds_read_b128 v[236:239], v1 offset:1024\n\t"
        "v_mfma_f32_16x16x32_bf16 v[152:155], v[128:131], v[240:243], v[152:155]\n\t"
        "v_cvt_pk_bf16_f32 v191, v191, v195\n\t"
        "s_waitcnt lgkmcnt(3)\n\t"
        "v_mfma_f32_16x16x32_bf16 v[160:163], v[132:135], v[244:247], v[160:163]\n\t"
        "ds_read_b128 v[240:243], v2 offset:1024\n\t"
        "v_mfma_f32_16x16x32_bf16 v[152:155], v[136:139], v[244:247], v[152:155]\n\t"
        "ds_write_b64 v253, v[188:189] offset:0\n\t"
        "v_mfma_f32_16x16x32_bf16 v[160:163], v[140:143], v[248:251], v[160:163]\n\t"
        "ds_read_b128 v[244:247], v1 offset:2048\n\t"
        "v_mfma_f32_16x16x32_bf16 v[152:155], v[144:147], v[248:251], v[152:155]\n\t"
        "ds_write_b64 v254, v[190:191] offset:0\n\t"
        "s_waitcnt lgkmcnt(5)\n\t"
        "v_mfma_f32_16x16x32_bf16 v[176:179], v[4:7], v[228:231], 0\n\t"
        "ds_read_b128 v[248:251], v2 offset:2048\n\t"
        "v_mfma_f32_16x16x32_bf16 v[172:175], v[8:11], v[228:231], 0\n\t"
        "v_mfma_f32_16x16x32_bf16 v[176:179], v[12:15], v[232:235], v[176:179]\n\t"
        "ds_read_b128 v[228:231], v1 offset:3072\n\t"
        "v_mfma_f32_16x16x32_bf16 v[172:175], v[16:19], v[232:235], v[172:175]\n\t"
        "s_waitcnt lgkmcnt(5)\n\t"
        "v_mfma_f32_16x16x32_bf16 v[176:179], v[20:23], v[236:239], v[176:179]\n\t"
        "ds_read_b128 v[232:235], v2 offset:3072\n\t"
        "v_mfma_f32_16x16x32_bf16 v[172:175], v[24:27], v[236:239], v[172:175]\n\t"
        "v_cvt_pk_bf16_f32 v160, v160, v152\n\t"
        "v_mfma_f32_16x16x32_bf16 v[176:179], v[28:31], v[240:243], v[176:179]\n\t"
        "ds_read_b128 v[236:239], v1 offset:4096\n\t"
        "v_mfma_f32_16x16x32_bf16 v[172:175], v[32:35], v[240:243], v[172:175]\n\t"
        "v_cvt_pk_bf16_f32 v161, v161, v153\n\t"
        "s_waitcnt lgkmcnt(3)\n\t"
        "v_mfma_f32_16x16x32_bf16 v[176:179], v[36:39], v[244:247], v[176:179]\n\t"
        "ds_read_b128 v[240:243], v2 offset:4096\n\t"
        "v_mfma_f32_16x16x32_bf16 v[172:175], v[40:43], v[244:247], v[172:175]\n\t"
        "v_cvt_pk_bf16_f32 v162, v162, v154\n\t"
        "v_mfma_f32_16x16x32_bf16 v[176:179], v[44:47], v[248:251], v[176:179]\n\t"
        "ds_read_b128 v[244:247], v1 offset:5120\n\t"
        "v_mfma_f32_16x16x32_bf16 v[172:175], v[48:51], v[248:251], v[172:175]\n\t"
        "v_cvt_pk_bf16_f32 v163, v163, v155\n\t"
        "s_waitcnt lgkmcnt(3)\n\t"
        "v_mfma_f32_16x16x32_bf16 v[176:179], v[52:55], v[228:231], v[176:179]\n\t"
        "ds_read_b128 v[248:251], v2 offset:5120\n\t"
        "v_mfma_f32_16x16x32_bf16 v[172:175], v[56:59], v[228:231], v[172:175]\n\t"
        "ds_write_b64 v253, v[160:161] offset:4096\n\t"
        "v_mfma_f32_16x16x32_bf16 v[176:179], v[60:63], v[232:235], v[176:179]\n\t"
        "ds_read_b128 v[228:231], v1 offset:6144\n\t"
        "v_mfma_f32_16x16x32_bf16 v[172:175], v[64:67], v[232:235], v[172:175]\n\t"
        "ds_write_b64 v254, v[162:163] offset:4096\n\t"
        "s_waitcnt lgkmcnt(5)\n\t"
        "v_mfma_f32_16x16x32_bf16 v[176:179], v[68:71], v[236:239], v[176:179]\n\t"
        "ds_read_b128 v[232:235], v2 offset:6144\n\t"
        "v_mfma_f32_16x16x32_bf16 v[172:175], v[72:75], v[236:239], v[172:175]\n\t"
        "v_mfma_f32_16x16x32_bf16 v[176:179], v[76:79], v[240:243], v[176:179]\n\t"
        "ds_read_b128 v[236:239], v1 offset:7168\n\t"
        "v_mfma_f32_16x16x32_bf16 v[172:175], v[80:83], v[240:243], v[172:175]\n\t"
        "s_waitcnt lgkmcnt(5)\n\t"
        "v_mfma_f32_16x16x32_bf16 v[176:179], v[84:87], v[244:247], v[176:179]\n\t"
        "ds_read_b128 v[240:243], v2 offset:7168\n\t"
        "v_mfma_f32_16x16x32_bf16 v[172:175], v[88:91], v[244:247], v[172:175]\n\t"
        "v_mfma_f32_16x16x32_bf16 v[176:179], v[92:95], v[248:251], v[176:179]\n\t"
        "ds_read_b128 v[244:247], v1 offset:8192\n\t"
        "v_mfma_f32_16x16x32_bf16 v[172:175], v[96:99], v[248:251], v[172:175]\n\t"
        "s_waitcnt lgkmcnt(4)\n\t"
        "s_barrier\n\t"
        "s_waitcnt lgkmcnt(3)\n\t"
        "v_mfma_f32_16x16x32_bf16 v[176:179], v[100:103], v[228:231], v[176:179]\n\t"
        "ds_read_b128 v[248:251], v2 offset:8192\n\t"
        "v_mfma_f32_16x16x32_bf16 v[172:175], v[104:107], v[228:231], v[172:175]\n\t"
        "v_cvt_pk_bf16_f32 v196, v196, v200\n\t"
        "v_mfma_f32_16x16x32_bf16 v[176:179], v[108:111], v[232:235], v[176:179]\n\t"
        "v_add_u32 v1, 18432, v1\n\t"
        "v_add_u32 v2, 18432, v2\n\t"
        "ds_read_b128 v[228:231], v1 offset:0\n\t"
        "v_mfma_f32_16x16x32_bf16 v[172:175], v[112:115], v[232:235], v[172:175]\n\t"
        "v_cvt_pk_bf16_f32 v197, v197, v201\n\t"
        "s_waitcnt lgkmcnt(3)\n\t"
        "v_mfma_f32_16x16x32_bf16 v[176:179], v[116:119], v[236:239], v[176:179]\n\t"
        "ds_read_b128 v[232:235], v2 offset:0\n\t"
        "v_mfma_f32_16x16x32_bf16 v[172:175], v[120:123], v[236:239], v[172:175]\n\t"
        "v_cvt_pk_bf16_f32 v198, v198, v202\n\t"
        "v_mfma_f32_16x16x32_bf16 v[176:179], v[124:127], v[240:243], v[176:179]\n\t"
        "ds_read_b128 v[236:239], v1 offset:1024\n\t"
        "v_mfma_f32_16x16x32_bf16 v[172:175], v[128:131], v[240:243], v[172:175]\n\t"
        "v_cvt_pk_bf16_f32 v199, v199, v203\n\t"
        "s_waitcnt lgkmcnt(3)\n\t"
        "v_mfma_f32_16x16x32_bf16 v[176:179], v[132:135], v[244:247], v[176:179]\n\t"
        "ds_read_b128 v[240:243], v2 offset:1024\n\t"
        "v_mfma_f32_16x16x32_bf16 v[172:175], v[136:139], v[244:247], v[172:175]\n\t"
        "ds_write_b64 v253, v[196:197] offset:8192\n\t"
        "v_mfma_f32_16x16x32_bf16 v[176:179], v[140:143], v[248:251], v[176:179]\n\t"
        "ds_read_b128 v[244:247], v1 offset:2048\n\t"
        "v_mfma_f32_16x16x32_bf16 v[172:175], v[144:147], v[248:251], v[172:175]\n\t"
        "ds_write_b64 v254, v[198:199] offset:8192\n\t"
        "s_waitcnt lgkmcnt(5)\n\t"
        "v_mfma_f32_16x16x32_bf16 v[156:159], v[4:7], v[228:231], 0\n\t"
        "ds_read_b128 v[248:251], v2 offset:2048\n\t"
        "v_mfma_f32_16x16x32_bf16 v[148:151], v[8:11], v[228:231], 0\n\t"
        "v_mfma_f32_16x16x32_bf16 v[156:159], v[12:15], v[232:235], v[156:159]\n\t"
        "ds_read_b128 v[228:231], v1 offset:3072\n\t"
        "v_mfma_f32_16x16x32_bf16 v[148:151], v[16:19], v[232:235], v[148:151]\n\t"
        "s_waitcnt lgkmcnt(5)\n\t"
        "v_mfma_f32_16x16x32_bf16 v[156:159], v[20:23], v[236:239], v[156:159]\n\t"
        "ds_read_b128 v[232:235], v2 offset:3072\n\t"
        "v_mfma_f32_16x16x32_bf16 v[148:151], v[24:27], v[236:239], v[148:151]\n\t"
        "v_cvt_pk_bf16_f32 v176, v176, v172\n\t"
        "v_mfma_f32_16x16x32_bf16 v[156:159], v[28:31], v[240:243], v[156:159]\n\t"
        "ds_read_b128 v[236:239], v1 offset:4096\n\t"
        "v_mfma_f32_16x16x32_bf16 v[148:151], v[32:35], v[240:243], v[148:151]\n\t"
        "v_cvt_pk_bf16_f32 v177, v177, v173\n\t"
        "s_waitcnt lgkmcnt(3)\n\t"
        "v_mfma_f32_16x16x32_bf16 v[156:159], v[36:39], v[244:247], v[156:159]\n\t"
        "ds_read_b128 v[240:243], v2 offset:4096\n\t"
        "v_mfma_f32_16x16x32_bf16 v[148:151], v[40:43], v[244:247], v[148:151]\n\t"
        "v_cvt_pk_bf16_f32 v178, v178, v174\n\t"
        "v_mfma_f32_16x16x32_bf16 v[156:159], v[44:47], v[248:251], v[156:159]\n\t"
        "ds_read_b128 v[244:247], v1 offset:5120\n\t"
        "v_mfma_f32_16x16x32_bf16 v[148:151], v[48:51], v[248:251], v[148:151]\n\t"
        "v_cvt_pk_bf16_f32 v179, v179, v175\n\t"
        "s_waitcnt lgkmcnt(3)\n\t"
        "v_mfma_f32_16x16x32_bf16 v[156:159], v[52:55], v[228:231], v[156:159]\n\t"
        "ds_read_b128 v[248:251], v2 offset:5120\n\t"
        "v_mfma_f32_16x16x32_bf16 v[148:151], v[56:59], v[228:231], v[148:151]\n\t"
        "ds_write_b64 v253, v[176:177] offset:12288\n\t"
        "v_mfma_f32_16x16x32_bf16 v[156:159], v[60:63], v[232:235], v[156:159]\n\t"
        "ds_read_b128 v[228:231], v1 offset:6144\n\t"
        "v_mfma_f32_16x16x32_bf16 v[148:151], v[64:67], v[232:235], v[148:151]\n\t"
        "ds_write_b64 v254, v[178:179] offset:12288\n\t"
        "s_waitcnt lgkmcnt(5)\n\t"
        "v_mfma_f32_16x16x32_bf16 v[156:159], v[68:71], v[236:239], v[156:159]\n\t"
        "ds_read_b128 v[232:235], v2 offset:6144\n\t"
        "v_mfma_f32_16x16x32_bf16 v[148:151], v[72:75], v[236:239], v[148:151]\n\t"
        "v_mfma_f32_16x16x32_bf16 v[156:159], v[76:79], v[240:243], v[156:159]\n\t"
        "ds_read_b128 v[236:239], v1 offset:7168\n\t"
        "v_mfma_f32_16x16x32_bf16 v[148:151], v[80:83], v[240:243], v[148:151]\n\t"
        "s_waitcnt lgkmcnt(5)\n\t"
        "v_mfma_f32_16x16x32_bf16 v[156:159], v[84:87], v[244:247], v[156:159]\n\t"
        "ds_read_b128 v[240:243], v2 offset:7168\n\t"
        "v_mfma_f32_16x16x32_bf16 v[148:151], v[88:91], v[244:247], v[148:151]\n\t"
        "v_mfma_f32_16x16x32_bf16 v[156:159], v[92:95], v[248:251], v[156:159]\n\t"
        "ds_read_b128 v[244:247], v1 offset:8192\n\t"
        "v_mfma_f32_16x16x32_bf16 v[148:151], v[96:99], v[248:251], v[148:151]\n\t"
        "s_waitcnt lgkmcnt(4)\n\t"
        "s_barrier\n\t"
        "s_waitcnt lgkmcnt(3)\n\t"
        "v_mfma_f32_16x16x32_bf16 v[156:159], v[100:103], v[228:231], v[156:159]\n\t"
        "ds_read_b128 v[248:251], v2 offset:8192\n\t"
        "v_mfma_f32_16x16x32_bf16 v[148:151], v[104:107], v[228:231], v[148:151]\n\t"
        "v_cvt_pk_bf16_f32 v204, v204, v208\n\t"
        "v_mfma_f32_16x16x32_bf16 v[156:159], v[108:111], v[232:235], v[156:159]\n\t"
        "v_add_u32 v1, 18432, v1\n\t"
        "v_add_u32 v2, 18432, v2\n\t"
        "ds_read_b128 v[228:231], v1 offset:0\n\t"
        "v_mfma_f32_16x16x32_bf16 v[148:151], v[112:115], v[232:235], v[148:151]\n\t"
        "v_cvt_pk_bf16_f32 v205, v205, v209\n\t"
        "s_waitcnt lgkmcnt(3)\n\t"
        "v_mfma_f32_16x16x32_bf16 v[156:159], v[116:119], v[236:239], v[156:159]\n\t"
        "ds_read_b128 v[232:235], v2 offset:0\n\t"
        "v_mfma_f32_16x16x32_bf16 v[148:151], v[120:123], v[236:239], v[148:151]\n\t"
        "v_cvt_pk_bf16_f32 v206, v206, v210\n\t"
        "v_mfma_f32_16x16x32_bf16 v[156:159], v[124:127], v[240:243], v[156:159]\n\t"
        "ds_read_b128 v[236:239], v1 offset:1024\n\t"
        "v_mfma_f32_16x16x32_bf16 v[148:151], v[128:131], v[240:243], v[148:151]\n\t"
        "v_cvt_pk_bf16_f32 v207, v207, v211\n\t"
        "s_waitcnt lgkmcnt(3)\n\t"
        "v_mfma_f32_16x16x32_bf16 v[156:159], v[132:135], v[244:247], v[156:159]\n\t"
        "ds_read_b128 v[240:243], v2 offset:1024\n\t"
        "v_mfma_f32_16x16x32_bf16 v[148:151], v[136:139], v[244:247], v[148:151]\n\t"
        "ds_write_b64 v253, v[204:205] offset:0\n\t"
        "v_mfma_f32_16x16x32_bf16 v[156:159], v[140:143], v[248:251], v[156:159]\n\t"
        "ds_read_b128 v[244:247], v1 offset:2048\n\t"
        "v_mfma_f32_16x16x32_bf16 v[148:151], v[144:147], v[248:251], v[148:151]\n\t"
        "ds_write_b64 v254, v[206:207] offset:0\n\t"
        "s_waitcnt lgkmcnt(5)\n\t"
        "v_mfma_f32_16x16x32_bf16 v[184:187], v[4:7], v[228:231], 0\n\t"
        "ds_read_b128 v[248:251], v2 offset:2048\n\t"
        "v_mfma_f32_16x16x32_bf16 v[180:183], v[8:11], v[228:231], 0\n\t"
        "v_mfma_f32_16x16x32_bf16 v[184:187], v[12:15], v[232:235], v[184:187]\n\t"
        "ds_read_b128 v[228:231], v1 offset:3072\n\t"
        "v_mfma_f32_16x16x32_bf16 v[180:183], v[16:19], v[232:235], v[180:183]\n\t"
        "s_waitcnt lgkmcnt(5)\n\t"
        "v_mfma_f32_16x16x32_bf16 v[184:187], v[20:23], v[236:239], v[184:187]\n\t"
        "ds_read_b128 v[232:235], v2 offset:3072\n\t"
        "v_mfma_f32_16x16x32_bf16 v[180:183], v[24:27], v[236:239], v[180:183]\n\t"
        "v_cvt_pk_bf16_f32 v156, v156, v148\n\t"
        "v_mfma_f32_16x16x32_bf16 v[184:187], v[28:31], v[240:243], v[184:187]\n\t"
        "ds_read_b128 v[236:239], v1 offset:4096\n\t"
        "v_mfma_f32_16x16x32_bf16 v[180:183], v[32:35], v[240:243], v[180:183]\n\t"
        "v_cvt_pk_bf16_f32 v157, v157, v149\n\t"
        "s_waitcnt lgkmcnt(3)\n\t"
        "v_mfma_f32_16x16x32_bf16 v[184:187], v[36:39], v[244:247], v[184:187]\n\t"
        "ds_read_b128 v[240:243], v2 offset:4096\n\t"
        "v_mfma_f32_16x16x32_bf16 v[180:183], v[40:43], v[244:247], v[180:183]\n\t"
        "v_cvt_pk_bf16_f32 v158, v158, v150\n\t"
        "v_mfma_f32_16x16x32_bf16 v[184:187], v[44:47], v[248:251], v[184:187]\n\t"
        "ds_read_b128 v[244:247], v1 offset:5120\n\t"
        "v_mfma_f32_16x16x32_bf16 v[180:183], v[48:51], v[248:251], v[180:183]\n\t"
        "v_cvt_pk_bf16_f32 v159, v159, v151\n\t"
        "s_waitcnt lgkmcnt(3)\n\t"
        "v_mfma_f32_16x16x32_bf16 v[184:187], v[52:55], v[228:231], v[184:187]\n\t"
        "ds_read_b128 v[248:251], v2 offset:5120\n\t"
        "v_mfma_f32_16x16x32_bf16 v[180:183], v[56:59], v[228:231], v[180:183]\n\t"
        "ds_write_b64 v253, v[156:157] offset:4096\n\t"
        "v_mfma_f32_16x16x32_bf16 v[184:187], v[60:63], v[232:235], v[184:187]\n\t"
        "ds_read_b128 v[228:231], v1 offset:6144\n\t"
        "v_mfma_f32_16x16x32_bf16 v[180:183], v[64:67], v[232:235], v[180:183]\n\t"
        "ds_write_b64 v254, v[158:159] offset:4096\n\t"
        "s_waitcnt lgkmcnt(5)\n\t"
        "v_mfma_f32_16x16x32_bf16 v[184:187], v[68:71], v[236:239], v[184:187]\n\t"
        "ds_read_b128 v[232:235], v2 offset:6144\n\t"
        "v_mfma_f32_16x16x32_bf16 v[180:183], v[72:75], v[236:239], v[180:183]\n\t"
        "v_mfma_f32_16x16x32_bf16 v[184:187], v[76:79], v[240:243], v[184:187]\n\t"
        "ds_read_b128 v[236:239], v1 offset:7168\n\t"
        "v_mfma_f32_16x16x32_bf16 v[180:183], v[80:83], v[240:243], v[180:183]\n\t"
        "s_waitcnt lgkmcnt(5)\n\t"
        "v_mfma_f32_16x16x32_bf16 v[184:187], v[84:87], v[244:247], v[184:187]\n\t"
        "ds_read_b128 v[240:243], v2 offset:7168\n\t"
        "v_mfma_f32_16x16x32_bf16 v[180:183], v[88:91], v[244:247], v[180:183]\n\t"
        "v_mfma_f32_16x16x32_bf16 v[184:187], v[92:95], v[248:251], v[184:187]\n\t"
        "ds_read_b128 v[244:247], v1 offset:8192\n\t"
        "v_mfma_f32_16x16x32_bf16 v[180:183], v[96:99], v[248:251], v[180:183]\n\t"
        "s_waitcnt lgkmcnt(4)\n\t"
        "s_barrier\n\t"
        "s_waitcnt lgkmcnt(3)\n\t"
        "v_mfma_f32_16x16x32_bf16 v[184:187], v[100:103], v[228:231], v[184:187]\n\t"
        "ds_read_b128 v[248:251], v2 offset:8192\n\t"
        "v_mfma_f32_16x16x32_bf16 v[180:183], v[104:107], v[228:231], v[180:183]\n\t"
        "v_cvt_pk_bf16_f32 v212, v212, v216\n\t"
        "v_mfma_f32_16x16x32_bf16 v[184:187], v[108:111], v[232:235], v[184:187]\n\t"
        "v_add_u32 v1, 53248, v1\n\t"
        "v_add_u32 v2, 53248, v2\n\t"
        "ds_read_b128 v[228:231], v1 offset:0\n\t"
        "v_mfma_f32_16x16x32_bf16 v[180:183], v[112:115], v[232:235], v[180:183]\n\t"
        "v_cvt_pk_bf16_f32 v213, v213, v217\n\t"
        "s_waitcnt lgkmcnt(3)\n\t"
        "v_mfma_f32_16x16x32_bf16 v[184:187], v[116:119], v[236:239], v[184:187]\n\t"
        "ds_read_b128 v[232:235], v2 offset:0\n\t"
        "v_mfma_f32_16x16x32_bf16 v[180:183], v[120:123], v[236:239], v[180:183]\n\t"
        "v_cvt_pk_bf16_f32 v214, v214, v218\n\t"
        "v_mfma_f32_16x16x32_bf16 v[184:187], v[124:127], v[240:243], v[184:187]\n\t"
        "ds_read_b128 v[236:239], v1 offset:1024\n\t"
        "v_mfma_f32_16x16x32_bf16 v[180:183], v[128:131], v[240:243], v[180:183]\n\t"
        "v_cvt_pk_bf16_f32 v215, v215, v219\n\t"
        "s_waitcnt lgkmcnt(3)\n\t"
        "v_mfma_f32_16x16x32_bf16 v[184:187], v[132:135], v[244:247], v[184:187]\n\t"
        "ds_read_b128 v[240:243], v2 offset:1024\n\t"
        "v_mfma_f32_16x16x32_bf16 v[180:183], v[136:139], v[244:247], v[180:183]\n\t"
        "ds_write_b64 v253, v[212:213] offset:8192\n\t"
        "v_mfma_f32_16x16x32_bf16 v[184:187], v[140:143], v[248:251], v[184:187]\n\t"
        "ds_read_b128 v[244:247], v1 offset:2048\n\t"
        "v_mfma_f32_16x16x32_bf16 v[180:183], v[144:147], v[248:251], v[180:183]\n\t"
        "ds_write_b64 v254, v[214:215] offset:8192\n\t"
        "s_cmp_eq_u32 s60, 19\n\t"
        "s_cbranch_scc1 L_c19_%=\n\t"
        "s_waitcnt lgkmcnt(5)\n\t"
        "v_mfma_f32_16x16x32_bf16 v[164:167], v[4:7], v[228:231], 0\n\t"
        "ds_read_b128 v[248:251], v2 offset:2048\n\t"
        "v_mfma_f32_16x16x32_bf16 v[168:171], v[8:11], v[228:231], 0\n\t"
        "v_mfma_f32_16x16x32_bf16 v[164:167], v[12:15], v[232:235], v[164:167]\n\t"
        "ds_read_b128 v[228:231], v1 offset:3072\n\t"
        "v_mfma_f32_16x16x32_bf16 v[168:171], v[16:19], v[232:235], v[168:171]\n\t"
        "s_waitcnt lgkmcnt(5)\n\t"
        "v_mfma_f32_16x16x32_bf16 v[164:167], v[20:23], v[236:239], v[164:167]\n\t"
        "ds_read_b128 v[232:235], v2 offset:3072\n\t"
        "v_mfma_f32_16x16x32_bf16 v[168:171], v[24:27], v[236:239], v[168:171]\n\t"
        "v_cvt_pk_bf16_f32 v184, v184, v180\n\t"
        "v_mfma_f32_16x16x32_bf16 v[164:167], v[28:31], v[240:243], v[164:167]\n\t"
        "ds_read_b128 v[236:239], v1 offset:4096\n\t"
        "v_mfma_f32_16x16x32_bf16 v[168:171], v[32:35], v[240:243], v[168:171]\n\t"
        "v_cvt_pk_bf16_f32 v185, v185, v181\n\t"
        "s_waitcnt lgkmcnt(3)\n\t"
        "v_mfma_f32_16x16x32_bf16 v[164:167], v[36:39], v[244:247], v[164:167]\n\t"
        "ds_read_b128 v[240:243], v2 offset:4096\n\t"
        "v_mfma_f32_16x16x32_bf16 v[168:171], v[40:43], v[244:247], v[168:171]\n\t"
        "v_cvt_pk_bf16_f32 v186, v186, v182\n\t"
        "v_mfma_f32_16x16x32_bf16 v[164:167], v[44:47], v[248:251], v[164:167]\n\t"
        "ds_read_b128 v[244:247], v1 offset:5120\n\t"
        "v_mfma_f32_16x16x32_bf16 v[168:171], v[48:51], v[248:251], v[168:171]\n\t"
        "v_cvt_pk_bf16_f32 v187, v187, v183\n\t"
        "s_waitcnt lgkmcnt(3)\n\t"
        "v_mfma_f32_16x16x32_bf16 v[164:167], v[52:55], v[228:231], v[164:167]\n\t"
        "ds_read_b128 v[248:251], v2 offset:5120\n\t"
        "v_mfma_f32_16x16x32_bf16 v[168:171], v[56:59], v[228:231], v[168:171]\n\t"
        "ds_write_b64 v253, v[184:185] offset:12288\n\t"
        "v_mfma_f32_16x16x32_bf16 v[164:167], v[60:63], v[232:235], v[164:167]\n\t"
        "ds_read_b128 v[228:231], v1 offset:6144\n\t"
        "v_mfma_f32_16x16x32_bf16 v[168:171], v[64:67], v[232:235], v[168:171]\n\t"
        "ds_write_b64 v254, v[186:187] offset:12288\n\t"
        "s_waitcnt lgkmcnt(5)\n\t"
        "v_mfma_f32_16x16x32_bf16 v[164:167], v[68:71], v[236:239], v[164:167]\n\t"
        "ds_read_b128 v[232:235], v2 offset:6144\n\t"
        "v_mfma_f32_16x16x32_bf16 v[168:171], v[72:75], v[236:239], v[168:171]\n\t"
        "v_mfma_f32_16x16x32_bf16 v[164:167], v[76:79], v[240:243], v[164:167]\n\t"
        "ds_read_b128 v[236:239], v1 offset:7168\n\t"
        "v_mfma_f32_16x16x32_bf16 v[168:171], v[80:83], v[240:243], v[168:171]\n\t"
        "s_waitcnt lgkmcnt(5)\n\t"
        "v_mfma_f32_16x16x32_bf16 v[164:167], v[84:87], v[244:247], v[164:167]\n\t"
        "ds_read_b128 v[240:243], v2 offset:7168\n\t"
        "v_mfma_f32_16x16x32_bf16 v[168:171], v[88:91], v[244:247], v[168:171]\n\t"
        "v_mfma_f32_16x16x32_bf16 v[164:167], v[92:95], v[248:251], v[164:167]\n\t"
        "ds_read_b128 v[244:247], v1 offset:8192\n\t"
        "v_mfma_f32_16x16x32_bf16 v[168:171], v[96:99], v[248:251], v[168:171]\n\t"
        "s_waitcnt lgkmcnt(4)\n\t"
        "s_barrier\n\t"
        "s_waitcnt lgkmcnt(3)\n\t"
        "v_mfma_f32_16x16x32_bf16 v[164:167], v[100:103], v[228:231], v[164:167]\n\t"
        "ds_read_b128 v[248:251], v2 offset:8192\n\t"
        "v_mfma_f32_16x16x32_bf16 v[168:171], v[104:107], v[228:231], v[168:171]\n\t"
        "v_cvt_pk_bf16_f32 v220, v220, v224\n\t"
        "v_mfma_f32_16x16x32_bf16 v[164:167], v[108:111], v[232:235], v[164:167]\n\t"
        "v_mfma_f32_16x16x32_bf16 v[168:171], v[112:115], v[232:235], v[168:171]\n\t"
        "v_cvt_pk_bf16_f32 v221, v221, v225\n\t"
        "s_waitcnt lgkmcnt(2)\n\t"
        "v_mfma_f32_16x16x32_bf16 v[164:167], v[116:119], v[236:239], v[164:167]\n\t"
        "v_mfma_f32_16x16x32_bf16 v[168:171], v[120:123], v[236:239], v[168:171]\n\t"
        "v_cvt_pk_bf16_f32 v222, v222, v226\n\t"
        "v_mfma_f32_16x16x32_bf16 v[164:167], v[124:127], v[240:243], v[164:167]\n\t"
        "v_mfma_f32_16x16x32_bf16 v[168:171], v[128:131], v[240:243], v[168:171]\n\t"
        "v_cvt_pk_bf16_f32 v223, v223, v227\n\t"
        "s_waitcnt lgkmcnt(0)\n\t"
        "v_mfma_f32_16x16x32_bf16 v[164:167], v[132:135], v[244:247], v[164:167]\n\t"
        "v_mfma_f32_16x16x32_bf16 v[168:171], v[136:139], v[244:247], v[168:171]\n\t"
        "ds_write_b64 v253, v[220:221] offset:0\n\t"
        "v_mfma_f32_16x16x32_bf16 v[164:167], v[140:143], v[248:251], v[164:167]\n\t"
        "v_mfma_f32_16x16x32_bf16 v[168:171], v[144:147], v[248:251], v[168:171]\n\t"
        "ds_write_b64 v254, v[222:223] offset:0\n\t"
        "s_nop 7\n\t"
        "v_cvt_pk_bf16_f32 v164, v164, v168\n\t"
        "v_cvt_pk_bf16_f32 v165, v165, v169\n\t"
        "v_cvt_pk_bf16_f32 v166, v166, v170\n\t"
        "v_cvt_pk_bf16_f32 v167, v167, v171\n\t"
        "ds_write_b64 v253, v[164:165] offset:4096\n\t"
        "ds_write_b64 v254, v[166:167] offset:4096\n\t"
        "s_waitcnt lgkmcnt(0)\n\t"
        "s_barrier\n\t"
        "s_endpgm\n\t"
        "L_c19_%=:\n\t"
        "s_waitcnt lgkmcnt(5)\n\t"
        "v_mfma_f32_16x16x32_bf16 v[164:167], v[4:7], v[228:231], 0\n\t"
        "ds_read_b128 v[248:251], v2 offset:2048\n\t"
        "v_mfma_f32_16x16x32_bf16 v[168:171], v[8:11], v[228:231], 0\n\t"
        "v_mfma_f32_16x16x32_bf16 v[164:167], v[12:15], v[232:235], v[164:167]\n\t"
        "ds_read_b128 v[228:231], v1 offset:3072\n\t"
        "v_mfma_f32_16x16x32_bf16 v[168:171], v[16:19], v[232:235], v[168:171]\n\t"
        "s_waitcnt lgkmcnt(5)\n\t"
        "v_mfma_f32_16x16x32_bf16 v[164:167], v[20:23], v[236:239], v[164:167]\n\t"
        "ds_read_b128 v[232:235], v2 offset:3072\n\t"
        "v_mfma_f32_16x16x32_bf16 v[168:171], v[24:27], v[236:239], v[168:171]\n\t"
        "v_cvt_pk_bf16_f32 v184, v184, v180\n\t"
        "v_mfma_f32_16x16x32_bf16 v[164:167], v[28:31], v[240:243], v[164:167]\n\t"
        "ds_read_b128 v[236:239], v1 offset:4096\n\t"
        "v_mfma_f32_16x16x32_bf16 v[168:171], v[32:35], v[240:243], v[168:171]\n\t"
        "v_cvt_pk_bf16_f32 v185, v185, v181\n\t"
        "s_waitcnt lgkmcnt(3)\n\t"
        "v_mfma_f32_16x16x32_bf16 v[164:167], v[36:39], v[244:247], v[164:167]\n\t"
        "ds_read_b128 v[240:243], v2 offset:4096\n\t"
        "v_mfma_f32_16x16x32_bf16 v[168:171], v[40:43], v[244:247], v[168:171]\n\t"
        "v_cvt_pk_bf16_f32 v186, v186, v182\n\t"
        "v_mfma_f32_16x16x32_bf16 v[164:167], v[44:47], v[248:251], v[164:167]\n\t"
        "ds_read_b128 v[244:247], v1 offset:5120\n\t"
        "v_mfma_f32_16x16x32_bf16 v[168:171], v[48:51], v[248:251], v[168:171]\n\t"
        "v_cvt_pk_bf16_f32 v187, v187, v183\n\t"
        "s_waitcnt lgkmcnt(3)\n\t"
        "v_mfma_f32_16x16x32_bf16 v[164:167], v[52:55], v[228:231], v[164:167]\n\t"
        "ds_read_b128 v[248:251], v2 offset:5120\n\t"
        "v_mfma_f32_16x16x32_bf16 v[168:171], v[56:59], v[228:231], v[168:171]\n\t"
        "ds_write_b64 v253, v[184:185] offset:12288\n\t"
        "v_mfma_f32_16x16x32_bf16 v[164:167], v[60:63], v[232:235], v[164:167]\n\t"
        "ds_read_b128 v[228:231], v1 offset:6144\n\t"
        "v_mfma_f32_16x16x32_bf16 v[168:171], v[64:67], v[232:235], v[168:171]\n\t"
        "ds_write_b64 v254, v[186:187] offset:12288\n\t"
        "s_waitcnt lgkmcnt(5)\n\t"
        "v_mfma_f32_16x16x32_bf16 v[164:167], v[68:71], v[236:239], v[164:167]\n\t"
        "ds_read_b128 v[232:235], v2 offset:6144\n\t"
        "v_mfma_f32_16x16x32_bf16 v[168:171], v[72:75], v[236:239], v[168:171]\n\t"
        "v_mfma_f32_16x16x32_bf16 v[164:167], v[76:79], v[240:243], v[164:167]\n\t"
        "ds_read_b128 v[236:239], v1 offset:7168\n\t"
        "v_mfma_f32_16x16x32_bf16 v[168:171], v[80:83], v[240:243], v[168:171]\n\t"
        "s_waitcnt lgkmcnt(5)\n\t"
        "v_mfma_f32_16x16x32_bf16 v[164:167], v[84:87], v[244:247], v[164:167]\n\t"
        "ds_read_b128 v[240:243], v2 offset:7168\n\t"
        "v_mfma_f32_16x16x32_bf16 v[168:171], v[88:91], v[244:247], v[168:171]\n\t"
        "v_mfma_f32_16x16x32_bf16 v[164:167], v[92:95], v[248:251], v[164:167]\n\t"
        "ds_read_b128 v[244:247], v1 offset:8192\n\t"
        "v_mfma_f32_16x16x32_bf16 v[168:171], v[96:99], v[248:251], v[168:171]\n\t"
        "s_waitcnt lgkmcnt(4)\n\t"
        "s_barrier\n\t"
        "s_waitcnt lgkmcnt(3)\n\t"
        "v_mfma_f32_16x16x32_bf16 v[164:167], v[100:103], v[228:231], v[164:167]\n\t"
        "ds_read_b128 v[248:251], v2 offset:8192\n\t"
        "v_mfma_f32_16x16x32_bf16 v[168:171], v[104:107], v[228:231], v[168:171]\n\t"
        "v_cvt_pk_bf16_f32 v220, v220, v224\n\t"
        "v_mfma_f32_16x16x32_bf16 v[164:167], v[108:111], v[232:235], v[164:167]\n\t"
        "v_add_u32 v1, 18432, v1\n\t"
        "v_add_u32 v2, 18432, v2\n\t"
        "ds_read_b128 v[228:231], v1 offset:0\n\t"
        "v_mfma_f32_16x16x32_bf16 v[168:171], v[112:115], v[232:235], v[168:171]\n\t"
        "v_cvt_pk_bf16_f32 v221, v221, v225\n\t"
        "s_waitcnt lgkmcnt(3)\n\t"
        "v_mfma_f32_16x16x32_bf16 v[164:167], v[116:119], v[236:239], v[164:167]\n\t"
        "ds_read_b128 v[232:235], v2 offset:0\n\t"
        "v_mfma_f32_16x16x32_bf16 v[168:171], v[120:123], v[236:239], v[168:171]\n\t"
        "v_cvt_pk_bf16_f32 v222, v222, v226\n\t"
        "v_mfma_f32_16x16x32_bf16 v[164:167], v[124:127], v[240:243], v[164:167]\n\t"
        "ds_read_b128 v[236:239], v1 offset:1024\n\t"
        "v_mfma_f32_16x16x32_bf16 v[168:171], v[128:131], v[240:243], v[168:171]\n\t"
        "v_cvt_pk_bf16_f32 v223, v223, v227\n\t"
        "s_waitcnt lgkmcnt(3)\n\t"
        "v_mfma_f32_16x16x32_bf16 v[164:167], v[132:135], v[244:247], v[164:167]\n\t"
        "ds_read_b128 v[240:243], v2 offset:1024\n\t"
        "v_mfma_f32_16x16x32_bf16 v[168:171], v[136:139], v[244:247], v[168:171]\n\t"
        "ds_write_b64 v253, v[220:221] offset:0\n\t"
        "v_mfma_f32_16x16x32_bf16 v[164:167], v[140:143], v[248:251], v[164:167]\n\t"
        "ds_read_b128 v[244:247], v1 offset:2048\n\t"
        "v_mfma_f32_16x16x32_bf16 v[168:171], v[144:147], v[248:251], v[168:171]\n\t"
        "ds_write_b64 v254, v[222:223] offset:0\n\t"
        "s_waitcnt lgkmcnt(5)\n\t"
        "v_mfma_f32_16x16x32_bf16 v[192:195], v[4:7], v[228:231], 0\n\t"
        "ds_read_b128 v[248:251], v2 offset:2048\n\t"
        "v_mfma_f32_16x16x32_bf16 v[188:191], v[8:11], v[228:231], 0\n\t"
        "v_mfma_f32_16x16x32_bf16 v[192:195], v[12:15], v[232:235], v[192:195]\n\t"
        "ds_read_b128 v[228:231], v1 offset:3072\n\t"
        "v_mfma_f32_16x16x32_bf16 v[188:191], v[16:19], v[232:235], v[188:191]\n\t"
        "s_waitcnt lgkmcnt(5)\n\t"
        "v_mfma_f32_16x16x32_bf16 v[192:195], v[20:23], v[236:239], v[192:195]\n\t"
        "ds_read_b128 v[232:235], v2 offset:3072\n\t"
        "v_mfma_f32_16x16x32_bf16 v[188:191], v[24:27], v[236:239], v[188:191]\n\t"
        "v_cvt_pk_bf16_f32 v164, v164, v168\n\t"
        "v_mfma_f32_16x16x32_bf16 v[192:195], v[28:31], v[240:243], v[192:195]\n\t"
        "ds_read_b128 v[236:239], v1 offset:4096\n\t"
        "v_mfma_f32_16x16x32_bf16 v[188:191], v[32:35], v[240:243], v[188:191]\n\t"
        "v_cvt_pk_bf16_f32 v165, v165, v169\n\t"
        "s_waitcnt lgkmcnt(3)\n\t"
        "v_mfma_f32_16x16x32_bf16 v[192:195], v[36:39], v[244:247], v[192:195]\n\t"
        "ds_read_b128 v[240:243], v2 offset:4096\n\t"
        "v_mfma_f32_16x16x32_bf16 v[188:191], v[40:43], v[244:247], v[188:191]\n\t"
        "v_cvt_pk_bf16_f32 v166, v166, v170\n\t"
        "v_mfma_f32_16x16x32_bf16 v[192:195], v[44:47], v[248:251], v[192:195]\n\t"
        "ds_read_b128 v[244:247], v1 offset:5120\n\t"
        "v_mfma_f32_16x16x32_bf16 v[188:191], v[48:51], v[248:251], v[188:191]\n\t"
        "v_cvt_pk_bf16_f32 v167, v167, v171\n\t"
        "s_waitcnt lgkmcnt(3)\n\t"
        "v_mfma_f32_16x16x32_bf16 v[192:195], v[52:55], v[228:231], v[192:195]\n\t"
        "ds_read_b128 v[248:251], v2 offset:5120\n\t"
        "v_mfma_f32_16x16x32_bf16 v[188:191], v[56:59], v[228:231], v[188:191]\n\t"
        "ds_write_b64 v253, v[164:165] offset:4096\n\t"
        "v_mfma_f32_16x16x32_bf16 v[192:195], v[60:63], v[232:235], v[192:195]\n\t"
        "ds_read_b128 v[228:231], v1 offset:6144\n\t"
        "v_mfma_f32_16x16x32_bf16 v[188:191], v[64:67], v[232:235], v[188:191]\n\t"
        "ds_write_b64 v254, v[166:167] offset:4096\n\t"
        "s_waitcnt lgkmcnt(5)\n\t"
        "v_mfma_f32_16x16x32_bf16 v[192:195], v[68:71], v[236:239], v[192:195]\n\t"
        "ds_read_b128 v[232:235], v2 offset:6144\n\t"
        "v_mfma_f32_16x16x32_bf16 v[188:191], v[72:75], v[236:239], v[188:191]\n\t"
        "v_mfma_f32_16x16x32_bf16 v[192:195], v[76:79], v[240:243], v[192:195]\n\t"
        "ds_read_b128 v[236:239], v1 offset:7168\n\t"
        "v_mfma_f32_16x16x32_bf16 v[188:191], v[80:83], v[240:243], v[188:191]\n\t"
        "s_waitcnt lgkmcnt(5)\n\t"
        "v_mfma_f32_16x16x32_bf16 v[192:195], v[84:87], v[244:247], v[192:195]\n\t"
        "ds_read_b128 v[240:243], v2 offset:7168\n\t"
        "v_mfma_f32_16x16x32_bf16 v[188:191], v[88:91], v[244:247], v[188:191]\n\t"
        "v_mfma_f32_16x16x32_bf16 v[192:195], v[92:95], v[248:251], v[192:195]\n\t"
        "ds_read_b128 v[244:247], v1 offset:8192\n\t"
        "v_mfma_f32_16x16x32_bf16 v[188:191], v[96:99], v[248:251], v[188:191]\n\t"
        "s_waitcnt lgkmcnt(4)\n\t"
        "s_barrier\n\t"
        "s_waitcnt lgkmcnt(3)\n\t"
        "v_mfma_f32_16x16x32_bf16 v[192:195], v[100:103], v[228:231], v[192:195]\n\t"
        "ds_read_b128 v[248:251], v2 offset:8192\n\t"
        "v_mfma_f32_16x16x32_bf16 v[188:191], v[104:107], v[228:231], v[188:191]\n\t"
        "v_mfma_f32_16x16x32_bf16 v[192:195], v[108:111], v[232:235], v[192:195]\n\t"
        "v_mfma_f32_16x16x32_bf16 v[188:191], v[112:115], v[232:235], v[188:191]\n\t"
        "s_waitcnt lgkmcnt(2)\n\t"
        "v_mfma_f32_16x16x32_bf16 v[192:195], v[116:119], v[236:239], v[192:195]\n\t"
        "v_mfma_f32_16x16x32_bf16 v[188:191], v[120:123], v[236:239], v[188:191]\n\t"
        "v_mfma_f32_16x16x32_bf16 v[192:195], v[124:127], v[240:243], v[192:195]\n\t"
        "v_mfma_f32_16x16x32_bf16 v[188:191], v[128:131], v[240:243], v[188:191]\n\t"
        "s_waitcnt lgkmcnt(0)\n\t"
        "v_mfma_f32_16x16x32_bf16 v[192:195], v[132:135], v[244:247], v[192:195]\n\t"
        "v_mfma_f32_16x16x32_bf16 v[188:191], v[136:139], v[244:247], v[188:191]\n\t"
        "v_mfma_f32_16x16x32_bf16 v[192:195], v[140:143], v[248:251], v[192:195]\n\t"
        "v_mfma_f32_16x16x32_bf16 v[188:191], v[144:147], v[248:251], v[188:191]\n\t"
        "s_nop 7\n\t"
        "v_cvt_pk_bf16_f32 v192, v192, v188\n\t"
        "v_cvt_pk_bf16_f32 v193, v193, v189\n\t"
        "v_cvt_pk_bf16_f32 v194, v194, v190\n\t"
        "v_cvt_pk_bf16_f32 v195, v195, v191\n\t"
        "ds_write_b64 v253, v[192:193] offset:8192\n\t"
        "ds_write_b64 v254, v[194:195] offset:8192\n\t"
        "s_waitcnt lgkmcnt(0)\n\t"
        "s_barrier\n\t"
        "s_endpgm\n\t"
        "L_end_%=:\n\t"
        : "+{v0}"(tid)
        : [ka] "{s[0:1]}"(ka), [blk] "{s2}"(blockIdx.x), [lds] "s"(lds0)
        : "memory", "vcc", "v1", "v2", "v3", "v4", "v5", "v6", "v7", "v8", "v9", "v10", "v11", "v12", "v13", "v14", "v15", "v16", "v17", "v18", "v19", "v20", "v21", "v22", "v23", "v24", "v25", "v26", "v27", "v28", "v29", "v30", "v31", "v32", "v33", "v34", "v35", "v36", "v37", "v38", "v39", "v40", "v41", "v42", "v43", "v44", "v45", "v46", "v47", "v48", "v49", "v50", "v51", "v52", "v53", "v54", "v55", "v56", "v57", "v58", "v59", "v60", "v61", "v62", "v63", "v64", "v65", "v66", "v67", "v68", "v69", "v70", "v71", "v72", "v73", "v74", "v75", "v76", "v77", "v78", "v79", "v80", "v81", "v82", "v83", "v84", "v85", "v86", "v87", "v88", "v89", "v90", "v91", "v92", "v93", "v94", "v95", "v96", "v97", "v98", "v99", "v100", "v101", "v102", "v103", "v104", "v105", "v106", "v107", "v108", "v109", "v110", "v111", "v112", "v113", "v114", "v115", "v116", "v117", "v118", "v119", "v120", "v121", "v122", "v123", "v124", "v125", "v126", "v127", "v128", "v129", "v130", "v131", "v132", "v133", "v134", "v135", "v136", "v137", "v138", "v139", "v140", "v141", "v142", "v143", "v144", "v145", "v146", "v147", "v148", "v149", "v150", "v151", "v152", "v153", "v154", "v155", "v156", "v157", "v158", "v159", "v160", "v161", "v162", "v163", "v164", "v165", "v166", "v167", "v168", "v169", "v170", "v171", "v172", "v173", "v174", "v175", "v176", "v177", "v178", "v179", "v180", "v181", "v182", "v183", "v184", "v185", "v186", "v187", "v188", "v189", "v190", "v191", "v192", "v193", "v194", "v195", "v196", "v197", "v198", "v199", "v200", "v201", "v202", "v203", "v204", "v205", "v206", "v207", "v208", "v209", "v210", "v211", "v212", "v213", "v214", "v215", "v216", "v217", "v218", "v219", "v220", "v221", "v222", "v223", "v224", "v225", "v226", "v227", "v228", "v229", "v230", "v231", "v232", "v233", "v234", "v235", "v236", "v237", "v238", "v239", "v240", "v241", "v242", "v243", "v244", "v245", "v246", "v247", "v248", "v249", "v250", "v251", "v252", "v253", "v254", "v255", "s4", "s5", "s6", "s7", "s8", "s9", "s10", "s11", "s12", "s13", "s14", "s15", "s16", "s17", "s18", "s19", "s20", "s21", "s22", "s23", "s24", "s25", "s26", "s27", "s28", "s29", "s30", "s31", "s36", "s37", "s38", "s39", "s40", "s41", "s42", "s43", "s44", "s45", "s46", "s47", "s48", "s49", "s50", "s51", "s52", "s53", "s54", "s55", "s56", "s57", "s58", "s59", "s60", "s61", "s62", "s63", "s64", "s65", "s66", "s67", "s68", "s69", "s70", "s71", "s72", "s73", "s74", "s75", "s76", "s77", "s78", "s79", "s80", "s81", "s82", "s83", "s84", "s85", "s86", "s87", "s88", "s89", "s90", "s91", "s92", "s93", "s94", "s95", "s96", "s97");
}

extern "C" void kernel_launch(void* const* d_in, const int* in_sizes, int n_in,
                              void* d_out, int out_size, void* d_ws, size_t ws_size,
                              hipStream_t stream) {
    wq_gemm_kernel<<<252, 512, 0, stream>>>(d_in[0], d_in[1], d_in[2], d_in[3], d_out);
}
```
